```python
import torch

def _softmax(x, axis=-1):
    return torch.softmax(x, dim=axis)
def _gelu(x):
    return torch.nn.functional.gelu(x, approximate="tanh")
def _silu(x):
    return torch.nn.functional.silu(x)
def _sigmoid(x):
    return torch.sigmoid(x)
def _layernorm(x, axis=-1, eps=1e-5):
    m = x.mean(dim=axis, keepdim=True)
    v = x.var(dim=axis, keepdim=True, unbiased=False)
    return (x - m) / torch.sqrt(v + eps)
def _scatter_add(tgt, idx, src, axis=0):
    return tgt.clone().index_add_(axis, idx, src)
def _segment_sum(src, seg, n):
    out = torch.zeros((n,) + src.shape[1:], dtype=src.dtype)
    return out.index_add_(0, seg, src)
def _pad(x, pw):
    flat = tuple(v for pair in reversed(pw) for v in pair)
    return torch.nn.functional.pad(x, flat)

kernel_name = 'parallel_sum__grouped_kv_mqa__dense_activation_squared_relu__prompt'

def setup_inputs():

    _w = (lambda a: (lambda t: t.to(torch.bfloat16) if t.is_floating_point() else t)(torch.as_tensor(a)))
    def _A_setup():
        B, S, D, H = 1, 2048, 512, 32
        Dh = D // H
        return {
            "x": _w((lambda *s: torch.randn(*s, dtype=torch.bfloat16) * 0.05)(B, S, D)),
            "wq": _w((lambda *s: torch.randn(*s, dtype=torch.bfloat16) * 0.2)(D, H * Dh)),
            "wk": _w((lambda *s: torch.randn(*s, dtype=torch.bfloat16) * 0.2)(D, Dh)),
            "wv": _w((lambda *s: torch.randn(*s, dtype=torch.bfloat16) * 0.1)(D, Dh)),
            "wo": _w((lambda *s: torch.randn(*s, dtype=torch.bfloat16) * 0.1)(H * Dh, D)),
        }
    _A_in = _A_setup()

    def _B_setup():
        B, S, D, F = 1, 2048, 512, 1376
        return {
            "x": _w((lambda *s: torch.randn(*s, dtype=torch.bfloat16) * 0.05)(B, S, D)),
            "w1": _w((lambda *s: torch.randn(*s, dtype=torch.bfloat16) * 0.05)(D, F)),
            "b1": _w((lambda *s: torch.randn(*s, dtype=torch.bfloat16) * 1.6)(F)),
            "w2": _w((lambda *s: torch.randn(*s, dtype=torch.bfloat16) * 0.05)(F, D)),
            "b2": _w((lambda *s: torch.randn(*s, dtype=torch.bfloat16) * 6.4)(D)),
        }
    _B_in = _B_setup()

    return {
        "A_x": _A_in["x"],
        "A_wq": _A_in["wq"],
        "A_wk": _A_in["wk"],
        "A_wv": _A_in["wv"],
        "A_wo": _A_in["wo"],
        "B_x": _B_in["x"],
        "B_w1": _B_in["w1"],
        "B_b1": _B_in["b1"],
        "B_w2": _B_in["w2"],
        "B_b2": _B_in["b2"],
    }

def reference(A_x, A_wq, A_wk, A_wv, A_wo, B_x, B_w1, B_b1, B_w2, B_b2):
    def _A_ref(x, wq, wk, wv, wo):
        B, S, D = x.shape
        H = 32
        Dh = D // H
        q = (lambda x, *d: x.permute(*d))((x @ wq).reshape(B, S, H, Dh), 0, 2, 1, 3)
        k = x @ wk
        v = x @ wv
        scores = torch.einsum("bhqd,bkd->bhqk", q, k) * (Dh ** -0.5)
        p = _softmax(scores, axis=-1)
        o = torch.einsum("bhqk,bkd->bhqd", p, v)
        return (lambda x, *d: x.permute(*d))(o, 0, 2, 1, 3).reshape(B, S, H * Dh) @ wo

    def _B_ref(x, w1, b1, w2, b2):
        h = torch.einsum("bsd,df->bsf", x, w1) + b1
        zero = torch.zeros_like(h)
        r = torch.where(h > zero, h, zero)
        h = r * r
        out = torch.einsum("bsf,fd->bsd", h, w2) + b2
        return out

    x = A_x
    xn = _layernorm(x, axis=-1)
    return x + _A_ref(xn, A_wq, A_wk, A_wv, A_wo) + _B_ref(xn, B_w1, B_b1, B_w2, B_b2)
```

```cpp
#include <hip/hip_runtime.h>
#include <hip/hip_bf16.h>
#include <stdint.h>

#define DI __device__ __forceinline__
#define LAS __attribute__((address_space(3)))
#define GAS __attribute__((address_space(1)))
typedef __attribute__((ext_vector_type(8))) __bf16 bf16x8;
typedef __attribute__((ext_vector_type(16))) __bf16 bf16x16;
typedef __attribute__((ext_vector_type(2))) __bf16 bf16x2;
typedef __attribute__((ext_vector_type(4))) short s16x4;
typedef __attribute__((ext_vector_type(8))) short s16x8;
typedef __attribute__((ext_vector_type(16))) float f32x16;
typedef __attribute__((ext_vector_type(8))) float f32x8;
typedef __attribute__((ext_vector_type(4))) float f32x4;
typedef __attribute__((ext_vector_type(2))) float f32x2;
typedef __attribute__((ext_vector_type(4))) unsigned u32x4;
typedef __attribute__((ext_vector_type(2))) unsigned u32x2;
typedef unsigned short u16;

constexpr int S_ = 2048, D_ = 512, F_ = 1376;
constexpr int KS_ACT = 118;
constexpr size_t WS_Q   = 0;
constexpr size_t WS_KV  = 2u << 20;
constexpr size_t WS_ACT = 3u << 20;
constexpr size_t WS_WT  = 11u << 20;
constexpr int KV_V0 = 65536, KV_VSTR = 1056, KV_BYTES = 131 * 1024;

#define MFMA32(a, b, c) __builtin_amdgcn_mfma_f32_32x32x16_bf16((a), (b), (c), 0, 0, 0)

DI unsigned pk2(float lo, float hi) { f32x2 v = {lo, hi}; return __builtin_bit_cast(unsigned, __builtin_convertvector(v, bf16x2)); }
DI float bf2f(u16 v) { return __uint_as_float((unsigned)v << 16); }
DI void glds16(const void* g, unsigned lds) {
  asm volatile("s_mov_b32 m0, %1\n\ts_nop 0\n\tglobal_load_lds_dwordx4 %0, off" :: "v"(g), "s"(lds) : "memory");
}
DI float dot2(unsigned a, unsigned b, float c) {
  return __builtin_amdgcn_fdot2_f32_bf16(__builtin_bit_cast(bf16x2, a), __builtin_bit_cast(bf16x2, b), c, false);
}
DI float xhalf(float v) {
  return __shfl_xor(v, 32);
}

constexpr int K1_STAGE = 32768, K1_STAT = 131072, K1_CS = K1_STAT + 4096, K1_LDS = K1_CS + 1024;

__global__ void __launch_bounds__(512, 2)
k1_proj(const u16* __restrict__ x, const u16* __restrict__ wq, const u16* __restrict__ wk, const u16* __restrict__ wv,
        const u16* __restrict__ wo, const u16* __restrict__ w1, const u16* __restrict__ b1, const u16* __restrict__ w2,
        char* __restrict__ ws) {
  extern __shared__ __attribute__((aligned(16))) char smem[];
  const int tid = threadIdx.x, lane = tid & 63, r = lane & 31, h = lane >> 5;
  const int w = __builtin_amdgcn_readfirstlane(tid >> 6);
  const int b = blockIdx.x, xcd = b & 7, bi = b >> 3;

  const int fr = b * 8 + w;
  u16 rt[8];
  if (fr < 16 * KS_ACT) {
    const int nt = fr & 15, ks = fr >> 4;
    const u16* src = (ks < 32 ? wo + (size_t)ks * 16 * D_ : w2 + (size_t)(ks - 32) * 16 * D_) + nt * 32 + r;
#pragma unroll
    for (int j = 0; j < 8; ++j) rt[j] = src[(8 * (j >> 2) + 4 * h + (j & 3)) * D_];
  }

  if (bi < 30) {
    const int mt = 2 * xcd + (bi >= 15 ? 1 : 0), ct = bi >= 15 ? bi - 15 : bi;
    const int wm = w & 1, wn = w >> 1;
    const unsigned lds0 = (unsigned)(uintptr_t)(LAS char*)smem;
    const char* xs[2]; const char* wsrc[2]; unsigned wstep[2];
#pragma unroll
    for (int i = 0; i < 2; ++i) {
      const int p = 2 * w + i;
      const int row = 8 * p + (lane >> 3), c = (lane & 7) ^ ((row >> 1) & 7);
      xs[i] = (const char*)x + (size_t)(mt * 128 + row) * (D_ * 2) + c * 16;
      const int k = 4 * p + (lane >> 4), cw = (lane & 15) ^ ((k & 3) << 2);
      if (ct < 4)                  { wsrc[i] = (const char*)wq + (size_t)k * (D_ * 2) + ct * 256 + cw * 16; wstep[i] = 64 * D_ * 2; }
      else if (ct < 14 || cw < 12) { wsrc[i] = (const char*)w1 + (size_t)k * (F_ * 2) + (ct - 4) * 256 + cw * 16; wstep[i] = 64 * F_ * 2; }
      else                         { wsrc[i] = (const char*)(cw < 14 ? wk : wv) + k * 32 + (cw & 1) * 16; wstep[i] = 64 * 32; }
    }
    auto stage = [&](int kc) {
      const unsigned base = lds0 + (kc & 3) * K1_STAGE + w * 2048;
      glds16(xs[0] + kc * 128, base);
      glds16(xs[1] + kc * 128, base + 1024);
      glds16(wsrc[0] + (size_t)kc * wstep[0], base + 16384);
      glds16(wsrc[1] + (size_t)kc * wstep[1], base + 16384 + 1024);
    };
    stage(0); stage(1); stage(2); stage(3);

    const int f = wn ^ (wm << 1);
    const int q4 = (lane & 15) >> 2, p4 = lane & 3, g = lane >> 4;
    int xa[4], wa[4];
#pragma unroll
    for (int i = 0; i < 4; ++i) {
      const int ks = i ^ f;
      xa[i] = (64 * wm + r) * 128 + (((2 * ks + h) ^ ((r >> 1) & 7)) << 4);
      wa[i] = 16384 + (16 * ks + 8 * h + q4) * 256 + (((4 * wn + 2 * (g & 1) + (p4 >> 1)) ^ (q4 << 2)) << 4) + (p4 & 1) * 8;
    }
    f32x16 acc0, acc1, av0, av1;
#pragma unroll
    for (int i = 0; i < 16; ++i) { acc0[i] = 0.f; acc1[i] = 0.f; av0[i] = 0.f; av1[i] = 0.f; }
    float s1a = 0.f, s1b = 0.f, s2a = 0.f, s2b = 0.f, cs = 0.f;
    const bool kvwave = (ct == 14) && (wn == 3);
    const unsigned ONES = 0x3f803f80u;

#pragma unroll
    for (int kc = 0; kc < 8; ++kc) {
      if (kc == 0)      asm volatile("s_waitcnt vmcnt(12)\n\ts_barrier" ::: "memory");
      else if (kc < 6)  asm volatile("s_waitcnt vmcnt(8) lgkmcnt(0)\n\ts_barrier" ::: "memory");
      else if (kc == 6) asm volatile("s_waitcnt vmcnt(4) lgkmcnt(0)\n\ts_barrier" ::: "memory");
      else              asm volatile("s_waitcnt vmcnt(0) lgkmcnt(0)\n\ts_barrier" ::: "memory");
      if (kc >= 1 && kc <= 4) stage(kc + 3);
      const char* sb = smem + (kc & 3) * K1_STAGE;
#pragma unroll
      for (int i = 0; i < 4; ++i) {
        const u32x4 x0 = *(const u32x4*)(sb + xa[i]);
        const u32x4 x1 = *(const u32x4*)(sb + xa[i] + 4096);
        const s16x4 wl = __builtin_amdgcn_ds_read_tr16_b64_v4i16((LAS s16x4*)(sb + wa[i]));
        const s16x4 wh = __builtin_amdgcn_ds_read_tr16_b64_v4i16((LAS s16x4*)(sb + wa[i] + 1024));
        const s16x8 wf = __builtin_shufflevector(wl, wh, 0, 1, 2, 3, 4, 5, 6, 7);
        const bf16x8 wb = __builtin_bit_cast(bf16x8, wf), xb0 = __builtin_bit_cast(bf16x8, x0), xb1 = __builtin_bit_cast(bf16x8, x1);
        acc0 = MFMA32(wb, xb0, acc0);
        acc1 = MFMA32(wb, xb1, acc1);
        if (kvwave) { av0 = MFMA32(xb0, wb, av0); av1 = MFMA32(xb1, wb, av1); }
        if (i == 0) {
#pragma unroll
          for (int d = 0; d < 4; ++d) { s1a = dot2(x0[d], ONES, s1a); s2a = dot2(x0[d], x0[d], s2a); s1b = dot2(x1[d], ONES, s1b); s2b = dot2(x1[d], x1[d], s2b); }
        }
        if (i < 2) {
          const u32x4 wu = __builtin_bit_cast(u32x4, wf);
#pragma unroll
          for (int d = 0; d < 4; ++d) cs = dot2(wu[d], ONES, cs);
        }
      }
    }
    s1a += xhalf(s1a); s2a += xhalf(s2a); s1b += xhalf(s1b); s2b += xhalf(s2b); cs += xhalf(cs);
    f32x2* stat = (f32x2*)(smem + K1_STAT);
    float* csp = (float*)(smem + K1_CS);
    {
      const f32x2 v = h ? f32x2{s1b, s2b} : f32x2{s1a, s2a};
      stat[(64 * wm + 32 * h + r) * 4 + wn] = v;
      if (h == 0) csp[wm * 128 + 32 * wn + r] = cs;
    }
    __syncthreads();
    auto rowstat = [&](int tok, float& rs, float& nmr) {
      const f32x4 a = *(const f32x4*)&stat[tok * 4], c = *(const f32x4*)&stat[tok * 4 + 2];
      const float mu = (a[0] + a[2] + c[0] + c[2]) * (1.f / D_);
      const float var = fmaxf((a[1] + a[3] + c[1] + c[3]) * (1.f / D_) - mu * mu, 0.f);
      rs = __builtin_amdgcn_rsqf(var + 1e-5f);
      nmr = -mu * rs;
    };
    float csr[16];
#pragma unroll
    for (int gq = 0; gq < 4; ++gq) {
      const f32x4 a = *(const f32x4*)&csp[32 * wn + 8 * gq + 4 * h], c = *(const f32x4*)&csp[128 + 32 * wn + 8 * gq + 4 * h];
#pragma unroll
      for (int j = 0; j < 4; ++j) csr[4 * gq + j] = a[j] + c[j];
    }
    const int tt0 = mt * 4 + wm * 2;
    if (ct < 4) {
      const float c0 = 0.25f * 1.44269504088896340736f;
#pragma unroll
      for (int tt = 0; tt < 2; ++tt) {
        float rs, nmr; rowstat(64 * wm + 32 * tt + r, rs, nmr); rs *= c0; nmr *= c0;
        const f32x16& a = tt ? acc1 : acc0;
#pragma unroll
        for (int s = 0; s < 2; ++s) {
          u32x4 o;
#pragma unroll
          for (int j = 0; j < 4; ++j) o[j] = pk2(fmaf(a[8 * s + 2 * j], rs, nmr * csr[8 * s + 2 * j]), fmaf(a[8 * s + 2 * j + 1], rs, nmr * csr[8 * s + 2 * j + 1]));
          *(u32x4*)(ws + WS_Q + ((size_t)((tt0 + tt) * 32 + ct * 8 + wn * 2 + s) * 64 + lane) * 16) = o;
        }
      }
    } else if (!kvwave) {
      const int n0 = (ct - 4) * 128 + 32 * wn;
      float bias[16];
#pragma unroll
      for (int gq = 0; gq < 4; ++gq) {
        const u32x2 bb = *(const u32x2*)(b1 + n0 + 8 * gq + 4 * h);
        bias[4 * gq] = __uint_as_float(bb[0] << 16); bias[4 * gq + 1] = __uint_as_float(bb[0] & 0xffff0000u);
        bias[4 * gq + 2] = __uint_as_float(bb[1] << 16); bias[4 * gq + 3] = __uint_as_float(bb[1] & 0xffff0000u);
      }
#pragma unroll
      for (int tt = 0; tt < 2; ++tt) {
        float rs, nmr; rowstat(64 * wm + 32 * tt + r, rs, nmr);
        const f32x16& a = tt ? acc1 : acc0;
#pragma unroll
        for (int s = 0; s < 2; ++s) {
          u32x4 o;
#pragma unroll
          for (int j = 0; j < 4; ++j) {
            const int i0 = 8 * s + 2 * j;
            float u0 = fmaxf(fmaf(a[i0], rs, fmaf(nmr, csr[i0], bias[i0])), 0.f), u1 = fmaxf(fmaf(a[i0 + 1], rs, fmaf(nmr, csr[i0 + 1], bias[i0 + 1])), 0.f);
            o[j] = pk2(u0 * u0, u1 * u1);
          }
          *(u32x4*)(ws + WS_ACT + ((size_t)((tt0 + tt) * KS_ACT + 32 + (n0 >> 4) + s) * 64 + lane) * 16) = o;
        }
      }
    } else {
      const int sr = (r & 0x13) | ((r & 4) << 1) | ((r & 8) >> 1);
      const float csl = csp[96 + r] + csp[128 + 96 + r];
      const int d = r & 15, pos = d < 4 ? d : (d < 12 ? d + 4 : d - 8);
#pragma unroll
      for (int tt = 0; tt < 2; ++tt) {
        float rs, nmr; rowstat(64 * wm + 32 * tt + r, rs, nmr);
        const f32x16& a = tt ? acc1 : acc0;
        u32x4 o;
#pragma unroll
        for (int j = 0; j < 4; ++j) o[j] = pk2(fmaf(a[2 * j], rs, nmr * csr[2 * j]), fmaf(a[2 * j + 1], rs, nmr * csr[2 * j + 1]));
        *(u32x4*)(ws + WS_KV + (size_t)(tt0 + tt) * 1024 + (h * 32 + sr) * 16) = o;
        const f32x16& v = tt ? av1 : av0;
        char* vb = ws + WS_KV + KV_V0 + (size_t)(tt0 + tt) * KV_VSTR;
#pragma unroll
        for (int s = 0; s < 2; ++s) {
          float val[8];
#pragma unroll
          for (int j = 0; j < 8; ++j) {
            float rs2, nmr2; rowstat(64 * wm + 32 * tt + 16 * s + 8 * (j >> 2) + 4 * h + (j & 3), rs2, nmr2);
            val[j] = fmaf(v[8 * s + j], rs2, nmr2 * csl);
          }
          const u32x4 ov = {pk2(val[0], val[1]), pk2(val[2], val[3]), pk2(val[4], val[5]), pk2(val[6], val[7])};
          if (r >= 16) *(u32x4*)(vb + 16 + s * 528 + h * 256 + pos * 16) = ov;
          else if (lane == 0) *(u32x4*)(vb + s * 528) = u32x4{ONES, ONES, ONES, ONES};
        }
        if (lane == 1 && tt0 + tt == 63) *(u32x4*)(vb + KV_VSTR) = u32x4{ONES, ONES, ONES, ONES};
      }
    }
  }
  if (fr < 16 * KS_ACT) {
    const int nt = fr & 15, ks = fr >> 4;
    const u32x4 o = {rt[0] | ((unsigned)rt[1] << 16), rt[2] | ((unsigned)rt[3] << 16), rt[4] | ((unsigned)rt[5] << 16), rt[6] | ((unsigned)rt[7] << 16)};
    *(u32x4*)(ws + WS_WT + ((size_t)(nt * KS_ACT + ks) * 64 + lane) * 16) = o;
  }
}

DI f32x16 pv(const u32x4& v, const f32x16& p, int s, const f32x16& o) {
  const f32x8 ph = s ? __builtin_shufflevector(p, p, 8, 9, 10, 11, 12, 13, 14, 15) : __builtin_shufflevector(p, p, 0, 1, 2, 3, 4, 5, 6, 7);
  return __builtin_amdgcn_smfmac_f32_32x32x32_bf16(__builtin_bit_cast(bf16x8, v), __builtin_bit_cast(bf16x16, ph), o, 0xDDDD, 0, 0);
}
DI float max16(const f32x16& s) {
  float a = fmaxf(fmaxf(s[0], s[1]), s[2]);
#pragma unroll
  for (int i = 3; i < 15; i += 2) a = fmaxf(fmaxf(a, s[i]), s[i + 1]);
  return fmaxf(a, s[15]);
}

__global__ void __launch_bounds__(512, 2)
k2_attn(char* __restrict__ ws) {
  extern __shared__ __attribute__((aligned(16))) char smem[];
  const int tid = threadIdx.x, lane = tid & 63, r = lane & 31, h = lane >> 5;
  const int w = __builtin_amdgcn_readfirstlane(tid >> 6);
  const int b = blockIdx.x, xcd = b & 7, bi = b >> 3;
  const int rt = 4 * xcd + (bi >> 3), g = bi & 7;
  const int T = 2 * rt + (w & 1), head = 4 * g + (w >> 1);
  const unsigned lds0 = (unsigned)(uintptr_t)(LAS char*)smem;

  const u32x4 qu = *(const u32x4*)(ws + WS_Q + ((size_t)(T * 32 + head) * 64 + lane) * 16);
  asm volatile("" ::: "memory");
  const char* kvsrc = ws + WS_KV + lane * 16;
#pragma unroll
  for (int j = 0; j < 5; ++j) { const int idx = min(j * 8 + w, 32); const int p = idx < 16 ? idx : 48 + idx; glds16(kvsrc + p * 1024, lds0 + p * 1024); }
#pragma unroll
  for (int j = 0; j < 13; ++j) { const int idx = min(j * 8 + w, 97); const int p = idx < 48 ? 16 + idx : 33 + idx; glds16(kvsrc + p * 1024, lds0 + p * 1024); }
  const bf16x8 q = __builtin_bit_cast(bf16x8, qu);

  const int d = r & 15, pos = d < 4 ? d : (d < 12 ? d + 4 : d - 8);
  const int voff = KV_V0 + 16 + (r < 16 ? h * 256 + pos * 16 : ((r >= 20 && r < 28) ? -16 : 512));
  const char* kp = smem + lane * 16;
  const char* vp = smem + voff;
  auto ldk = [&](int t) { return __builtin_bit_cast(bf16x8, *(const u32x4*)(kp + t * 1024)); };
  auto ldv = [&](int t, int s) { return *(const u32x4*)(vp + t * KV_VSTR + s * 528); };

  asm volatile("s_waitcnt vmcnt(13)\n\ts_barrier" ::: "memory");

  f32x16 zero;
#pragma unroll
  for (int i = 0; i < 16; ++i) zero[i] = 0.f;
  float m = fmaxf(max16(MFMA32(ldk(0), q, zero)), max16(MFMA32(ldk(1), q, zero)));
  m = fmaxf(m, xhalf(m));
  f32x16 c;
#pragma unroll
  for (int i = 0; i < 16; ++i) c[i] = -(m + 90.f);
  f32x16 o = zero;

  f32x16 sa = MFMA32(ldk(0), q, c), sb;
  for (int t = 0; t < 64; t += 2) {
    if (t == 14) asm volatile("s_waitcnt vmcnt(0)\n\ts_barrier" ::: "memory");
    sb = MFMA32(ldk(t + 1), q, c);
    {
      const u32x4 v0 = ldv(t, 0), v1 = ldv(t, 1);
#pragma unroll
      for (int i = 0; i < 16; ++i) sa[i] = __builtin_amdgcn_exp2f(sa[i]);
      o = pv(v0, sa, 0, o); o = pv(v1, sa, 1, o);
    }
    sa = MFMA32(ldk((t + 2) & 63), q, c);
    {
      const u32x4 v0 = ldv(t + 1, 0), v1 = ldv(t + 1, 1);
#pragma unroll
      for (int i = 0; i < 16; ++i) sb[i] = __builtin_amdgcn_exp2f(sb[i]);
      o = pv(v0, sb, 0, o); o = pv(v1, sb, 1, o);
    }
  }
  {
    float z = 0.f;
#pragma unroll
    for (int i = 0; i < 8; ++i) z = fmaf(o[i], 0.f, z);
    const bool ok = (o[8] >= 0x1p-100f) && (o[8] <= 0x1p120f) && (z == 0.f);
    if (__builtin_amdgcn_ballot_w64(!ok) != 0) {
      float mx = -__builtin_inff();
      for (int t = 0; t < 64; ++t) mx = fmaxf(mx, max16(MFMA32(ldk(t), q, zero)));
      mx = fmaxf(mx, xhalf(mx));
#pragma unroll
      for (int i = 0; i < 16; ++i) c[i] = -mx;
      o = zero;
      for (int t = 0; t < 64; ++t) {
        f32x16 s = MFMA32(ldk(t), q, c);
#pragma unroll
        for (int i = 0; i < 16; ++i) s[i] = __builtin_amdgcn_exp2f(s[i]);
        o = pv(ldv(t, 0), s, 0, o); o = pv(ldv(t, 1), s, 1, o);
      }
    }
  }
  const float inv = 1.f / o[8];
  const u32x4 ou = {pk2(o[0] * inv, o[1] * inv), pk2(o[2] * inv, o[3] * inv), pk2(o[4] * inv, o[5] * inv), pk2(o[6] * inv, o[7] * inv)};
  *(u32x4*)(ws + WS_ACT + ((size_t)(T * KS_ACT + head) * 64 + lane) * 16) = ou;
}

__global__ void __launch_bounds__(512, 2)
k3_out(const u16* __restrict__ x, const u16* __restrict__ b2, const char* __restrict__ ws, u16* __restrict__ out) {
  __shared__ __attribute__((aligned(16))) float red[4][16][64];
  const int tid = threadIdx.x, lane = tid & 63, r = lane & 31, h = lane >> 5;
  const int w = __builtin_amdgcn_readfirstlane(tid >> 6);
  const int b = blockIdx.x, xcd = b & 7, bi = b >> 3;
  const int rt = 4 * xcd + (bi >> 3), g = bi & 7;
  const int T = 2 * rt + (w & 1), nt = 2 * g + ((w >> 1) & 1), kh = w >> 2;
  const u32x4* ap = (const u32x4*)(ws + WS_WT) + (size_t)(nt * KS_ACT + kh * 59) * 64 + lane;
  const u32x4* bp = (const u32x4*)(ws + WS_ACT) + (size_t)(T * KS_ACT + kh * 59) * 64 + lane;
  f32x16 acc;
#pragma unroll
  for (int i = 0; i < 16; ++i) acc[i] = 0.f;
  constexpr int PF = 8;
  u32x4 a[PF], bb[PF];
#pragma unroll
  for (int i = 0; i < PF; ++i) { a[i] = ap[i * 64]; bb[i] = bp[i * 64]; }
#pragma unroll
  for (int ks = 0; ks < 59; ++ks) {
    acc = MFMA32(__builtin_bit_cast(bf16x8, a[ks % PF]), __builtin_bit_cast(bf16x8, bb[ks % PF]), acc);
    if (ks + PF < 59) { a[ks % PF] = ap[(ks + PF) * 64]; bb[ks % PF] = bp[(ks + PF) * 64]; }
  }
  if (kh) {
#pragma unroll
    for (int i = 0; i < 16; ++i) red[w & 3][i][lane] = acc[i];
  }
  __syncthreads();
  if (!kh) {
    const size_t row = (size_t)(T * 32 + r) * D_ + nt * 32 + 4 * h;
#pragma unroll
    for (int gq = 0; gq < 4; ++gq) {
      const u32x2 xx = *(const u32x2*)(x + row + 8 * gq), bq = *(const u32x2*)(b2 + nt * 32 + 4 * h + 8 * gq);
      const float v0 = acc[4 * gq] + red[w][4 * gq][lane] + __uint_as_float(xx[0] << 16) + __uint_as_float(bq[0] << 16);
      const float v1 = acc[4 * gq + 1] + red[w][4 * gq + 1][lane] + __uint_as_float(xx[0] & 0xffff0000u) + __uint_as_float(bq[0] & 0xffff0000u);
      const float v2 = acc[4 * gq + 2] + red[w][4 * gq + 2][lane] + __uint_as_float(xx[1] << 16) + __uint_as_float(bq[1] << 16);
      const float v3 = acc[4 * gq + 3] + red[w][4 * gq + 3][lane] + __uint_as_float(xx[1] & 0xffff0000u) + __uint_as_float(bq[1] & 0xffff0000u);
      *(u32x2*)(out + row + 8 * gq) = u32x2{pk2(v0, v1), pk2(v2, v3)};
    }
  }
}

extern "C" void kernel_launch(void* const* d_in, const int* in_sizes, int n_in, void* d_out, int out_size, void* d_ws, size_t ws_size,
                              hipStream_t stream) {
  const u16* x = (const u16*)d_in[0]; const u16* wq = (const u16*)d_in[1]; const u16* wk = (const u16*)d_in[2];
  const u16* wv = (const u16*)d_in[3]; const u16* wo = (const u16*)d_in[4];
  const u16* w1 = (const u16*)d_in[6]; const u16* b1 = (const u16*)d_in[7]; const u16* w2 = (const u16*)d_in[8]; const u16* b2 = (const u16*)d_in[9];
  char* ws = (char*)d_ws;
  (void)hipFuncSetAttribute((const void*)k1_proj, hipFuncAttributeMaxDynamicSharedMemorySize, K1_LDS);
  (void)hipFuncSetAttribute((const void*)k2_attn, hipFuncAttributeMaxDynamicSharedMemorySize, KV_BYTES);
  k1_proj<<<256, 512, K1_LDS, stream>>>(x, wq, wk, wv, wo, w1, b1, w2, ws);
  k2_attn<<<256, 512, KV_BYTES, stream>>>(ws);
  k3_out<<<256, 512, 0, stream>>>(x, b2, ws, (u16*)d_out);
}
```

```cpp
#include <hip/hip_runtime.h>
#include <hip/hip_bf16.h>

__global__ void __launch_bounds__(512, 2)
fused_block(const void* x, const void* wq, const void* wk, const void* wv, const void* wo, const void* w1, const void* b1,
            const void* w2, const void* b2, void* ws, void* out) {
  unsigned tid = threadIdx.x;
  asm volatile(
    "s_load_dwordx2 s[40:41], s[0:1], 0x0\n"
    "s_load_dwordx2 s[44:45], s[0:1], 0x8\n"
    "s_load_dwordx2 s[48:49], s[0:1], 0x10\n"
    "s_load_dwordx2 s[52:53], s[0:1], 0x18\n"
    "s_load_dwordx2 s[56:57], s[0:1], 0x20\n"
    "s_load_dwordx2 s[60:61], s[0:1], 0x28\n"
    "s_load_dwordx2 s[64:65], s[0:1], 0x30\n"
    "s_load_dwordx2 s[68:69], s[0:1], 0x38\n"
    "s_load_dwordx2 s[72:73], s[0:1], 0x40\n"
    "s_load_dwordx2 s[76:77], s[0:1], 0x48\n"
    "s_load_dwordx2 s[80:81], s[0:1], 0x50\n"
    "v_lshrrev_b32 v1, 6, v0\n"
    "v_and_b32 v254, 63, v0\n"
    "v_readfirstlane_b32 s4, v1\n"
    "s_mov_b32 s42, 0x200000\n"
    "s_mov_b32 s43, 0x20000\n"
    "s_mov_b32 s46, 0x80000\n"
    "s_mov_b32 s47, 0x20000\n"
    "s_mov_b32 s50, 0x4000\n"
    "s_mov_b32 s51, 0x20000\n"
    "s_mov_b32 s54, 0x4000\n"
    "s_mov_b32 s55, 0x20000\n"
    "s_mov_b32 s58, 0x80000\n"
    "s_mov_b32 s59, 0x20000\n"
    "s_mov_b32 s62, 0x158000\n"
    "s_mov_b32 s63, 0x20000\n"
    "s_mov_b32 s66, 0xac0\n"
    "s_mov_b32 s67, 0x20000\n"
    "s_mov_b32 s70, 0x158000\n"
    "s_mov_b32 s71, 0x20000\n"
    "s_mov_b32 s74, 0x400\n"
    "s_mov_b32 s75, 0x20000\n"
    "s_mov_b32 s78, 0x7c0000\n"
    "s_mov_b32 s79, 0x20000\n"
    "s_mov_b32 s82, 0x200000\n"
    "s_mov_b32 s83, 0x20000\n"
    "s_bfe_u32 s21, s2, 0x20001\n"
    "s_lshr_b32 s22, s2, 5\n"
    "s_lshl3_add_u32 s5, s21, s22\n"
    "s_and_b32 s21, s2, 1\n"
    "s_bfe_u32 s22, s2, 0x20003\n"
    "s_lshl2_add_u32 s6, s21, s22\n"
    "s_lshl_b32 s10, s4, 10\n"
    "s_and_b32 s11, s4, 1\n"
    "s_lshr_b32 s12, s4, 1\n"
    "s_lshr_b32 s36, s4, 2\n"
    "s_cmp_gt_u32 s4, 4\n"
    "s_cbranch_scc1 pf1_%=\n"
    "s_getpc_b64 s[26:27]\n"
    "v_min_u32 v1, 250, v0\n"
    "v_lshlrev_b32 v1, 7, v1\n"
    "global_load_dword v251, v1, s[26:27]\n"
    "pf1_%=:\n"
    "v_lshrrev_b32 v1, 3, v254\n"
    "v_and_b32 v2, 7, v254\n"
    "v_lshrrev_b32 v3, 4, v254\n"
    "v_lshl_or_b32 v3, s11, 2, v3\n"
    "v_xor_b32 v3, v3, v2\n"
    "v_lshlrev_b32 v4, 10, v1\n"
    "v_lshl_or_b32 v112, v3, 4, v4\n"
    "v_and_b32 v3, 2, v1\n"
    "v_lshlrev_b32 v3, 1, v3\n"
    "v_xor_b32 v3, v3, v2\n"
    "v_lshl_or_b32 v253, v3, 4, v4\n"
    "s_lshl_b32 s21, s4, 13\n"
    "s_lshl_b32 s22, s5, 16\n"
    "s_add_u32 s15, s21, s22\n"
    "s_lshl_b32 s22, s6, 7\n"
    "s_add_u32 s16, s21, s22\n"
    "s_cmp_eq_u32 s6, 6\n"
    "s_cselect_b32 s90, 5, 6\n"
    "s_cmp_eq_u32 s6, 7\n"
    "s_cselect_b32 s13, 1, 0\n"
    "s_cselect_b32 s90, 2, s90\n"
    "s_mul_i32 s7, s6, 6\n"
    "s_sub_u32 s7, s7, s13\n"
    "s_lshl_b32 s23, s90, 2\n"
    "s_lshl_b32 s17, s7, 6\n"
    "s_lshl1_add_u32 s25, s6, s5\n"
    "s_and_b32 s25, s25, 7\n"
    "s_mul_i32 s22, s4, 3072\n"
    "s_mul_i32 s21, s4, 192\n"
    "s_movk_i32 s24, 2752\n"
    "v_add_u32 v1, s21, v254\n"
    "v_mul_u32_u24 v2, 2731, v1\n"
    "v_lshrrev_b32 v2, 16, v2\n"
    "v_mul_u32_u24 v3, 24, v2\n"
    "v_sub_u32 v1, v1, v3\n"
    "v_and_b32 v3, 2, v2\n"
    "v_lshlrev_b32 v3, 1, v3\n"
    "v_xor_b32 v1, v1, v3\n"
    "v_and_b32 v3, 7, v1\n"
    "v_cmp_le_u32 vcc, s23, v1\n"
    "s_nop 1\n"
    "v_cndmask_b32 v1, v1, v3, vcc\n"
    "v_lshlrev_b32 v1, 4, v1\n"
    "v_mad_u32_u24 v114, v2, s24, v1\n"
    "v_add_u32 v1, s21, v254\n"
    "v_add_u32 v1, 64, v1\n"
    "v_mul_u32_u24 v2, 2731, v1\n"
    "v_lshrrev_b32 v2, 16, v2\n"
    "v_mul_u32_u24 v3, 24, v2\n"
    "v_sub_u32 v1, v1, v3\n"
    "v_and_b32 v3, 2, v2\n"
    "v_lshlrev_b32 v3, 1, v3\n"
    "v_xor_b32 v1, v1, v3\n"
    "v_and_b32 v3, 7, v1\n"
    "v_cmp_le_u32 vcc, s23, v1\n"
    "s_nop 1\n"
    "v_cndmask_b32 v1, v1, v3, vcc\n"
    "v_lshlrev_b32 v1, 4, v1\n"
    "v_mad_u32_u24 v115, v2, s24, v1\n"
    "v_add_u32 v1, s21, v254\n"
    "v_add_u32 v1, 128, v1\n"
    "v_mul_u32_u24 v2, 2731, v1\n"
    "v_lshrrev_b32 v2, 16, v2\n"
    "v_mul_u32_u24 v3, 24, v2\n"
    "v_sub_u32 v1, v1, v3\n"
    "v_and_b32 v3, 2, v2\n"
    "v_lshlrev_b32 v3, 1, v3\n"
    "v_xor_b32 v1, v1, v3\n"
    "v_and_b32 v3, 7, v1\n"
    "v_cmp_le_u32 vcc, s23, v1\n"
    "s_nop 1\n"
    "v_cndmask_b32 v1, v1, v3, vcc\n"
    "v_lshlrev_b32 v1, 4, v1\n"
    "v_mad_u32_u24 v116, v2, s24, v1\n"
    "s_waitcnt lgkmcnt(0)\n"
    "s_cmp_lg_u32 s13, 0\n"
    "s_cbranch_scc1 kv2_%=\n"
    "s_add_u32 s28, s25, 0\n"
    "s_and_b32 s28, s28, 7\n"
    "s_lshl_b32 s28, s28, 7\n"
    "s_add_u32 m0, s10, 65536\n"
    "s_add_u32 s28, s28, s15\n"
    "buffer_load_dwordx4 v112, s[40:43], s28 offen lds\n"
    "s_add_u32 s29, s25, 0\n"
    "s_and_b32 s29, s29, 7\n"
    "s_lshl_b32 s28, s29, 16\n"
    "s_add_u32 m0, s10, 24576\n"
    "s_add_u32 s28, s28, s16\n"
    "buffer_load_dwordx4 v253, s[44:47], s28 offen lds\n"
    "s_mul_i32 s28, s29, 176128\n"
    "s_add_u32 m0, s22, 0\n"
    "s_add_u32 s28, s28, s17\n"
    "buffer_load_dwordx4 v114, s[60:63], s28 offen lds\n"
    "s_add_u32 m0, s22, 1024\n"
    "s_nop 0\n"
    "buffer_load_dwordx4 v115, s[60:63], s28 offen lds\n"
    "s_add_u32 m0, s22, 2048\n"
    "s_nop 0\n"
    "buffer_load_dwordx4 v116, s[60:63], s28 offen lds\n"
    "s_load_dwordx2 s[8:9], s[76:77], 0x0\n"
    "v_lshlrev_b32 v255, 4, v254\n"
    "s_lshl1_add_u32 s87, s5, s11\n"
    "v_and_b32 v1, 31, v254\n"
    "v_lshrrev_b32 v2, 5, v254\n"
    "v_bfe_u32 v3, v254, 2, 2\n"
    "v_and_b32 v4, 3, v254\n"
    "v_lshlrev_b32 v4, 3, v4\n"
    "v_and_b32 v5, 16, v254\n"
    "v_lshl_or_b32 v7, v5, 1, v4\n"
    "v_and_b32 v8, 2, v3\n"
    "v_lshlrev_b32 v8, 5, v8\n"
    "v_lshl_or_b32 v6, v2, 3, v3\n"
    "v_bfe_u32 v9, v254, 1, 3\n"
    "v_xor_b32 v9, v9, v2\n"
    "v_lshl_or_b32 v10, s11, 5, v1\n"
    "v_lshlrev_b32 v10, 7, v10\n"
    "v_lshl_or_b32 v9, v9, 4, v10\n"
    "v_add_u32 v117, 65536, v9\n"
    "v_xor_b32 v118, 32, v117\n"
    "v_xor_b32 v119, 64, v117\n"
    "v_xor_b32 v120, 0x60, v117\n"
    "v_lshrrev_b32 v9, 2, v5\n"
    "v_xor_b32 v9, v9, v6\n"
    "v_lshl_or_b32 v9, v9, 5, v4\n"
    "v_lshl_or_b32 v9, v5, 7, v9\n"
    "v_add_u32 v125, 32768, v9\n"
    "v_xor_b32 v126, 0x80, v125\n"
    "s_lshl_b32 s39, s12, 1\n"
    "s_sub_u32 s39, s39, 2\n"
    "s_lshl_b32 s21, s39, 6\n"
    "v_or_b32 v8, v8, v7\n"
    "v_mul_u32_u24 v9, 384, v6\n"
    "v_add3_u32 v9, v9, v8, s21\n"
    "v_lshl_or_b32 v10, v6, 7, v8\n"
    "v_add_u32 v10, 0x6000, v10\n"
    "s_cmp_eq_u32 s12, 0\n"
    "s_cselect_b64 s[26:27], -1, 0\n"
    "v_cndmask_b32 v121, v9, v10, s[26:27]\n"
    "v_lshl_or_b32 v9, v2, 2, v3\n"
    "v_lshl_or_b32 v9, v9, 7, v8\n"
    "s_lshl_b32 s21, s12, 5\n"
    "v_xor_b32 v9, s21, v9\n"
    "s_lshl_b32 s21, s12, 11\n"
    "v_add_u32 v236, s21, v9\n"
    "v_xor_b32 v237, 64, v236\n"
    "v_add_u32 v238, 0xc000, v236\n"
    "v_add_u32 v239, 0xc000, v237\n"
    "v_add_u32 v248, 0x1c000, v236\n"
    "v_add_u32 v249, 0x1c000, v237\n"
    "v_lshlrev_b32 v242, 3, v2\n"
    "v_lshl_or_b32 v240, v1, 10, v242\n"
    "v_add_u32 v241, 0x2000, v253\n"
    "v_and_b32 v9, 15, v254\n"
    "v_cmp_gt_u32 vcc, 2, v9\n"
    "v_mov_b32 v10, 0x3f803f80\n"
    "v_and_b32 v1, 14, v9\n"
    "v_cndmask_b32 v232, 0, v10, vcc\n"
    "v_cmp_eq_u32 vcc, 2, v1\n"
    "v_mov_b32 v233, v232\n"
    "v_and_b32 v9, 1, v9\n"
    "v_cndmask_b32 v234, 0, v10, vcc\n"
    "v_mul_u32_u24 v9, 0xaaaa, v9\n"
    "v_mov_b32 v235, v234\n"
    "v_add_u32 v124, 0x4444, v9\n"
    "v_mov_b32 v224, 0\n"
    "v_mov_b32 v225, 0\n"
    "v_mov_b32 v226, 0\n"
    "v_mov_b32 v227, 0\n"
    "v_mov_b32 v228, 0\n"
    "v_mov_b32 v229, 0\n"
    "v_mov_b32 v230, 0\n"
    "v_mov_b32 v231, 0\n"
    "s_cmp_eq_u32 s12, 0\n"
    "s_cselect_b32 s21, 1, 3\n"
    "s_lshl_b32 s21, s21, 12\n"
    "s_mul_i32 s23, s21, s11\n"
    "s_sub_u32 s21, s21, s23\n"
    "v_add_u32 v122, s21, v121\n"
    "v_add_u32 v121, s23, v121\n"
    "s_lshl_b32 s21, s4, 8\n"
    "s_add_u32 s21, s21, 159744\n"
    "v_and_b32 v1, 31, v254\n"
    "v_lshl_add_u32 v123, v1, 2, s21\n"
    "v_add_u32 v8, 0, v121\n"
    "v_add_u32 v10, 0, v122\n"
    "v_xor_b32 v9, 64, v8\n"
    "v_xor_b32 v11, 64, v10\n"
    "s_lshl_b32 s21, s11, 6\n"
    "v_xor_b32 v117, s21, v117\n"
    "v_xor_b32 v118, s21, v118\n"
    "v_xor_b32 v119, s21, v119\n"
    "v_xor_b32 v120, s21, v120\n"
    "v_and_b32 v1, 27, v254\n"
    "v_cmp_eq_u32 vcc, 16, v1\n"
    "v_mov_b32 v1, 0x3f803f80\n"
    "v_mov_b32 v76, 0xdddd\n"
    "v_cndmask_b32 v64, 0, v1, vcc\n"
    "v_mov_b32 v65, v64\n"
    "v_mov_b32 v66, v64\n"
    "v_mov_b32 v67, v64\n"
    "v_mov_b32 v68, v64\n"
    "v_mov_b32 v69, v64\n"
    "v_mov_b32 v70, v64\n"
    "v_mov_b32 v71, v64\n"
    "v_mov_b32 v72, v64\n"
    "v_mov_b32 v73, v64\n"
    "v_mov_b32 v74, v64\n"
    "v_mov_b32 v75, v64\n"
    "v_mov_b32 v24, 0\n"
    "v_mov_b32 v25, 0\n"
    "v_mov_b32 v26, 0\n"
    "v_mov_b32 v27, 0\n"
    "v_mov_b32 v28, 0\n"
    "v_mov_b32 v29, 0\n"
    "v_mov_b32 v30, 0\n"
    "v_mov_b32 v31, 0\n"
    "v_mov_b32 v32, 0\n"
    "v_mov_b32 v33, 0\n"
    "v_mov_b32 v34, 0\n"
    "v_mov_b32 v35, 0\n"
    "v_mov_b32 v36, 0\n"
    "v_mov_b32 v37, 0\n"
    "v_mov_b32 v38, 0\n"
    "v_mov_b32 v39, 0\n"
    "v_mov_b32 v40, 0\n"
    "v_mov_b32 v41, 0\n"
    "v_mov_b32 v42, 0\n"
    "v_mov_b32 v43, 0\n"
    "v_mov_b32 v44, 0\n"
    "v_mov_b32 v45, 0\n"
    "v_mov_b32 v46, 0\n"
    "v_mov_b32 v47, 0\n"
    "v_mov_b32 v48, 0\n"
    "v_mov_b32 v49, 0\n"
    "v_mov_b32 v50, 0\n"
    "v_mov_b32 v51, 0\n"
    "v_mov_b32 v52, 0\n"
    "v_mov_b32 v53, 0\n"
    "v_mov_b32 v54, 0\n"
    "v_mov_b32 v55, 0\n"
    "v_mov_b32 v56, 0\n"
    "v_mov_b32 v57, 0\n"
    "v_mov_b32 v58, 0\n"
    "v_mov_b32 v59, 0\n"
    "v_mov_b32 v60, 0\n"
    "v_mov_b32 v61, 0\n"
    "v_mov_b32 v62, 0\n"
    "v_mov_b32 v63, 0\n"
    "s_mul_i32 s19, s87, 122880\n"
    "s_add_u32 s19, s19, 262144\n"
    "s_mov_b32 s88, 0\n"
    "s_mov_b32 s14, 0x7f000000\n"
    "s_lshr_b32 s89, s2, 3\n"
    "s_lshl_b32 s89, s89, 12\n"
    "s_lshl_b32 s21, s36, 14\n"
    "v_add_u32 v77, s21, v255\n"
    "v_add_u32 v250, 0x10000, v77\n"
    "s_and_b32 s21, s4, 3\n"
    "s_lshl1_add_u32 s21, s21, s36\n"
    "s_lshl_b32 s21, s21, 10\n"
    "s_add_u32 s21, s21, 149504\n"
    "v_add_u32 v78, s21, v255\n"
    "v_xor_b32 v79, 0x400, v78\n"
    "s_mov_b32 s30, 0xffff\n"
    "s_mov_b32 s31, 0xffff\n"
    "s_lshl_b32 s35, s5, 9\n"
    "s_add_u32 s35, s35, 16384\n"
    "s_lshl3_add_u32 s21, s6, s4\n"
    "s_lshl3_add_u32 s34, s21, s35\n"
    "s_mul_i32 s86, s12, 22528\n"
    "s_add_u32 s86, s86, s19\n"
    "s_and_b32 s21, s4, 2\n"
    "s_add_u32 s21, s21, s36\n"
    "s_lshl2_add_u32 s21, s6, s21\n"
    "s_add_u32 s21, s21, 88\n"
    "s_lshl_b32 s21, s21, 10\n"
    "s_add_u32 s20, s21, s19\n"
    "s_mul_i32 s21, s4, 2304\n"
    "s_add_u32 s21, s21, 131072\n"
    "v_add_u32 v243, s21, v255\n"
    "v_lshl_add_u32 v244, v254, 2, s21\n"
    "s_xor_b32 s21, s4, 4\n"
    "s_mul_i32 s21, s21, 2304\n"
    "s_add_u32 s21, s21, 131072\n"
    "v_add_u32 v245, s21, v255\n"
    "v_lshl_add_u32 v246, v254, 2, s21\n"
    "v_and_b32 v247, 3, v254\n"
    "v_lshlrev_b32 v247, 2, v247\n"
    "v_add_u32 v247, 158720, v247\n"
    "v_lshlrev_b32 v127, 7, v254\n"
    "v_and_b32 v1, 31, v254\n"
    "v_lshlrev_b32 v1, 1, v1\n"
    "s_lshl_b32 s21, s39, 6\n"
    "s_add_u32 s21, s21, s17\n"
    "buffer_load_ushort v22, v1, s[64:67], s21 offen\n"
    "buffer_load_ushort v23, v1, s[64:67], s21 offen offset:64\n"
    "s_add_u32 s28, s25, 1\n"
    "s_and_b32 s28, s28, 7\n"
    "s_lshl_b32 s28, s28, 7\n"
    "s_add_u32 m0, s10, 73728\n"
    "s_add_u32 s28, s28, s15\n"
    "buffer_load_dwordx4 v112, s[40:43], s28 offen lds\n"
    "s_add_u32 s29, s25, 1\n"
    "s_and_b32 s29, s29, 7\n"
    "s_lshl_b32 s28, s29, 16\n"
    "s_add_u32 m0, s10, 155648\n"
    "s_add_u32 s28, s28, s16\n"
    "buffer_load_dwordx4 v253, s[44:47], s28 offen lds\n"
    "s_mul_i32 s28, s29, 176128\n"
    "s_add_u32 m0, s22, 131072\n"
    "s_add_u32 s28, s28, s17\n"
    "buffer_load_dwordx4 v114, s[60:63], s28 offen lds\n"
    "s_add_u32 m0, s22, 132096\n"
    "s_nop 0\n"
    "buffer_load_dwordx4 v115, s[60:63], s28 offen lds\n"
    "s_add_u32 m0, s22, 133120\n"
    "s_nop 0\n"
    "buffer_load_dwordx4 v116, s[60:63], s28 offen lds\n"
    "s_cmp_eq_u32 s12, 0\n"
    "s_cbranch_scc0 pf3_%=\n"
    "s_waitcnt vmcnt(7) lgkmcnt(0)\n"
    "s_barrier\n"
    "s_add_u32 s8, s8, 1\n"
    "s_addc_u32 s9, s9, 0\n"
    "s_add_u32 s28, s25, 2\n"
    "s_and_b32 s28, s28, 7\n"
    "s_lshl_b32 s28, s28, 7\n"
    "s_add_u32 m0, s10, 81920\n"
    "s_add_u32 s28, s28, s15\n"
    "buffer_load_dwordx4 v112, s[40:43], s28 offen lds\n"
    "ds_read_b128 v[80:83], v117 offset:0\n"
    "ds_read_b64_tr_b16 v[160:161], v8 offset:0\n"
    "ds_read_b64_tr_b16 v[162:163], v8 offset:512\n"
    "ds_read_b64_tr_b16 v[192:193], v9 offset:0\n"
    "ds_read_b64_tr_b16 v[194:195], v9 offset:512\n"
    "ds_read_b128 v[84:87], v118 offset:0\n"
    "ds_read_b64_tr_b16 v[164:165], v8 offset:2048\n"
    "ds_read_b64_tr_b16 v[166:167], v8 offset:2560\n"
    "ds_read_b64_tr_b16 v[196:197], v9 offset:2048\n"
    "s_add_u32 s29, s25, 2\n"
    "s_and_b32 s29, s29, 7\n"
    "s_lshl_b32 s28, s29, 16\n"
    "s_add_u32 m0, s10, 57344\n"
    "s_add_u32 s28, s28, s16\n"
    "buffer_load_dwordx4 v253, s[44:47], s28 offen lds\n"
    "ds_read_b64_tr_b16 v[198:199], v9 offset:2560\n"
    "ds_read_b128 v[88:91], v119 offset:0\n"
    "ds_read_b64_tr_b16 v[168:169], v10 offset:0\n"
    "ds_read_b64_tr_b16 v[170:171], v10 offset:512\n"
    "ds_read_b64_tr_b16 v[200:201], v11 offset:0\n"
    "ds_read_b64_tr_b16 v[202:203], v11 offset:512\n"
    "s_mul_i32 s28, s29, 176128\n"
    "s_add_u32 m0, s22, 32768\n"
    "s_add_u32 s28, s28, s17\n"
    "buffer_load_dwordx4 v114, s[60:63], s28 offen lds\n"
    "ds_read_b128 v[92:95], v120 offset:0\n"
    "ds_read_b64_tr_b16 v[172:173], v10 offset:2048\n"
    "ds_read_b64_tr_b16 v[174:175], v10 offset:2560\n"
    "ds_read_b64_tr_b16 v[204:205], v11 offset:2048\n"
    "ds_read_b64_tr_b16 v[206:207], v11 offset:2560\n"
    "s_add_u32 m0, s22, 33792\n"
    "s_nop 0\n"
    "buffer_load_dwordx4 v115, s[60:63], s28 offen lds\n"
    "v_add_u32 v8, 131072, v121\n"
    "v_add_u32 v10, 131072, v122\n"
    "v_xor_b32 v9, 64, v8\n"
    "v_xor_b32 v11, 64, v10\n"
    "s_add_u32 m0, s22, 34816\n"
    "s_nop 0\n"
    "buffer_load_dwordx4 v116, s[60:63], s28 offen lds\n"
    "s_waitcnt vmcnt(5) lgkmcnt(0)\n"
    "s_barrier\n"
    "v_smfmac_f32_16x16x64_bf16 v[224:227], v[232:235], v[160:167], v124\n"
    "s_add_u32 s28, s25, 3\n"
    "s_and_b32 s28, s28, 7\n"
    "s_lshl_b32 s28, s28, 7\n"
    "s_add_u32 m0, s10, 90112\n"
    "s_add_u32 s28, s28, s15\n"
    "buffer_load_dwordx4 v112, s[40:43], s28 offen lds\n"
    "ds_read_b128 v[96:99], v117 offset:8192\n"
    "ds_read_b64_tr_b16 v[176:177], v8 offset:0\n"
    "ds_read_b64_tr_b16 v[178:179], v8 offset:512\n"
    "v_mfma_f32_32x32x16_bf16 v[128:143], v[160:163], v[80:83], 0\n"
    "ds_read_b64_tr_b16 v[208:209], v9 offset:0\n"
    "ds_read_b64_tr_b16 v[210:211], v9 offset:512\n"
    "ds_read_b128 v[100:103], v118 offset:8192\n"
    "v_smfmac_f32_16x16x64_bf16 v[228:231], v[232:235], v[192:199], v124\n"
    "ds_read_b64_tr_b16 v[180:181], v8 offset:2048\n"
    "ds_read_b64_tr_b16 v[182:183], v8 offset:2560\n"
    "ds_read_b64_tr_b16 v[212:213], v9 offset:2048\n"
    "v_mfma_f32_32x32x16_bf16 v[144:159], v[192:195], v[80:83], 0\n"
    "s_add_u32 s29, s25, 3\n"
    "s_and_b32 s29, s29, 7\n"
    "s_lshl_b32 s28, s29, 16\n"
    "s_add_u32 m0, s10, 24576\n"
    "s_add_u32 s28, s28, s16\n"
    "buffer_load_dwordx4 v253, s[44:47], s28 offen lds\n"
    "v_mfma_f32_32x32x16_bf16 v[128:143], v[164:167], v[84:87], v[128:143]\n"
    "ds_read_b64_tr_b16 v[214:215], v9 offset:2560\n"
    "ds_read_b128 v[104:107], v119 offset:8192\n"
    "ds_read_b64_tr_b16 v[184:185], v10 offset:0\n"
    "v_mfma_f32_32x32x16_bf16 v[144:159], v[196:199], v[84:87], v[144:159]\n"
    "ds_read_b64_tr_b16 v[186:187], v10 offset:512\n"
    "ds_read_b64_tr_b16 v[216:217], v11 offset:0\n"
    "ds_read_b64_tr_b16 v[218:219], v11 offset:512\n"
    "s_mul_i32 s28, s29, 176128\n"
    "s_add_u32 m0, s22, 0\n"
    "s_add_u32 s28, s28, s17\n"
    "buffer_load_dwordx4 v114, s[60:63], s28 offen lds\n"
    "v_mfma_f32_32x32x16_bf16 v[128:143], v[168:171], v[88:91], v[128:143]\n"
    "ds_read_b128 v[108:111], v120 offset:8192\n"
    "ds_read_b64_tr_b16 v[188:189], v10 offset:2048\n"
    "ds_read_b64_tr_b16 v[190:191], v10 offset:2560\n"
    "v_mfma_f32_32x32x16_bf16 v[144:159], v[200:203], v[88:91], v[144:159]\n"
    "ds_read_b64_tr_b16 v[220:221], v11 offset:2048\n"
    "ds_read_b64_tr_b16 v[222:223], v11 offset:2560\n"
    "s_add_u32 m0, s22, 1024\n"
    "s_nop 0\n"
    "buffer_load_dwordx4 v115, s[60:63], s28 offen lds\n"
    "v_mfma_f32_32x32x16_bf16 v[128:143], v[172:175], v[92:95], v[128:143]\n"
    "v_add_u32 v8, 32768, v121\n"
    "v_add_u32 v10, 32768, v122\n"
    "v_xor_b32 v9, 64, v8\n"
    "v_xor_b32 v11, 64, v10\n"
    "v_mfma_f32_32x32x16_bf16 v[144:159], v[204:207], v[92:95], v[144:159]\n"
    "s_add_u32 m0, s22, 2048\n"
    "s_nop 0\n"
    "buffer_load_dwordx4 v116, s[60:63], s28 offen lds\n"
    "s_waitcnt vmcnt(5) lgkmcnt(0)\n"
    "s_barrier\n"
    "v_smfmac_f32_16x16x64_bf16 v[224:227], v[232:235], v[176:183], v124\n"
    "s_add_u32 s28, s25, 4\n"
    "s_and_b32 s28, s28, 7\n"
    "s_lshl_b32 s28, s28, 7\n"
    "s_add_u32 m0, s10, 98304\n"
    "s_add_u32 s28, s28, s15\n"
    "buffer_load_dwordx4 v112, s[40:43], s28 offen lds\n"
    "ds_read_b128 v[80:83], v117 offset:16384\n"
    "ds_read_b64_tr_b16 v[160:161], v8 offset:0\n"
    "ds_read_b64_tr_b16 v[162:163], v8 offset:512\n"
    "v_mfma_f32_32x32x16_bf16 v[128:143], v[176:179], v[96:99], v[128:143]\n"
    "ds_read_b64_tr_b16 v[192:193], v9 offset:0\n"
    "ds_read_b64_tr_b16 v[194:195], v9 offset:512\n"
    "ds_read_b128 v[84:87], v118 offset:16384\n"
    "v_smfmac_f32_16x16x64_bf16 v[228:231], v[232:235], v[208:215], v124\n"
    "ds_read_b64_tr_b16 v[164:165], v8 offset:2048\n"
    "ds_read_b64_tr_b16 v[166:167], v8 offset:2560\n"
    "ds_read_b64_tr_b16 v[196:197], v9 offset:2048\n"
    "v_mfma_f32_32x32x16_bf16 v[144:159], v[208:211], v[96:99], v[144:159]\n"
    "s_add_u32 s29, s25, 4\n"
    "s_and_b32 s29, s29, 7\n"
    "s_lshl_b32 s28, s29, 16\n"
    "s_add_u32 m0, s10, 155648\n"
    "s_add_u32 s28, s28, s16\n"
    "buffer_load_dwordx4 v253, s[44:47], s28 offen lds\n"
    "v_mfma_f32_32x32x16_bf16 v[128:143], v[180:183], v[100:103], v[128:143]\n"
    "ds_read_b64_tr_b16 v[198:199], v9 offset:2560\n"
    "ds_read_b128 v[88:91], v119 offset:16384\n"
    "ds_read_b64_tr_b16 v[168:169], v10 offset:0\n"
    "v_mfma_f32_32x32x16_bf16 v[144:159], v[212:215], v[100:103], v[144:159]\n"
    "ds_read_b64_tr_b16 v[170:171], v10 offset:512\n"
    "ds_read_b64_tr_b16 v[200:201], v11 offset:0\n"
    "ds_read_b64_tr_b16 v[202:203], v11 offset:512\n"
    "s_mul_i32 s28, s29, 176128\n"
    "s_add_u32 m0, s22, 131072\n"
    "s_add_u32 s28, s28, s17\n"
    "buffer_load_dwordx4 v114, s[60:63], s28 offen lds\n"
    "v_mfma_f32_32x32x16_bf16 v[128:143], v[184:187], v[104:107], v[128:143]\n"
    "ds_read_b128 v[92:95], v120 offset:16384\n"
    "ds_read_b64_tr_b16 v[172:173], v10 offset:2048\n"
    "ds_read_b64_tr_b16 v[174:175], v10 offset:2560\n"
    "v_mfma_f32_32x32x16_bf16 v[144:159], v[216:219], v[104:107], v[144:159]\n"
    "ds_read_b64_tr_b16 v[204:205], v11 offset:2048\n"
    "ds_read_b64_tr_b16 v[206:207], v11 offset:2560\n"
    "s_add_u32 m0, s22, 132096\n"
    "s_nop 0\n"
    "buffer_load_dwordx4 v115, s[60:63], s28 offen lds\n"
    "v_mfma_f32_32x32x16_bf16 v[128:143], v[188:191], v[108:111], v[128:143]\n"
    "v_add_u32 v8, 0, v121\n"
    "v_add_u32 v10, 0, v122\n"
    "v_xor_b32 v9, 64, v8\n"
    "v_xor_b32 v11, 64, v10\n"
    "v_mfma_f32_32x32x16_bf16 v[144:159], v[220:223], v[108:111], v[144:159]\n"
    "s_add_u32 m0, s22, 133120\n"
    "s_nop 0\n"
    "buffer_load_dwordx4 v116, s[60:63], s28 offen lds\n"
    "s_waitcnt vmcnt(5) lgkmcnt(0)\n"
    "s_barrier\n"
    "v_smfmac_f32_16x16x64_bf16 v[224:227], v[232:235], v[160:167], v124\n"
    "s_add_u32 s28, s25, 5\n"
    "s_and_b32 s28, s28, 7\n"
    "s_lshl_b32 s28, s28, 7\n"
    "s_add_u32 m0, s10, 106496\n"
    "s_add_u32 s28, s28, s15\n"
    "buffer_load_dwordx4 v112, s[40:43], s28 offen lds\n"
    "ds_read_b128 v[96:99], v117 offset:24576\n"
    "ds_read_b64_tr_b16 v[176:177], v8 offset:0\n"
    "ds_read_b64_tr_b16 v[178:179], v8 offset:512\n"
    "v_mfma_f32_32x32x16_bf16 v[128:143], v[160:163], v[80:83], v[128:143]\n"
    "ds_read_b64_tr_b16 v[208:209], v9 offset:0\n"
    "ds_read_b64_tr_b16 v[210:211], v9 offset:512\n"
    "ds_read_b128 v[100:103], v118 offset:24576\n"
    "v_smfmac_f32_16x16x64_bf16 v[228:231], v[232:235], v[192:199], v124\n"
    "ds_read_b64_tr_b16 v[180:181], v8 offset:2048\n"
    "ds_read_b64_tr_b16 v[182:183], v8 offset:2560\n"
    "ds_read_b64_tr_b16 v[212:213], v9 offset:2048\n"
    "v_mfma_f32_32x32x16_bf16 v[144:159], v[192:195], v[80:83], v[144:159]\n"
    "s_add_u32 s29, s25, 5\n"
    "s_and_b32 s29, s29, 7\n"
    "s_lshl_b32 s28, s29, 16\n"
    "s_add_u32 m0, s10, 57344\n"
    "s_add_u32 s28, s28, s16\n"
    "buffer_load_dwordx4 v253, s[44:47], s28 offen lds\n"
    "v_mfma_f32_32x32x16_bf16 v[128:143], v[164:167], v[84:87], v[128:143]\n"
    "ds_read_b64_tr_b16 v[214:215], v9 offset:2560\n"
    "ds_read_b128 v[104:107], v119 offset:24576\n"
    "ds_read_b64_tr_b16 v[184:185], v10 offset:0\n"
    "v_mfma_f32_32x32x16_bf16 v[144:159], v[196:199], v[84:87], v[144:159]\n"
    "ds_read_b64_tr_b16 v[186:187], v10 offset:512\n"
    "ds_read_b64_tr_b16 v[216:217], v11 offset:0\n"
    "ds_read_b64_tr_b16 v[218:219], v11 offset:512\n"
    "s_mul_i32 s28, s29, 176128\n"
    "s_add_u32 m0, s22, 32768\n"
    "s_add_u32 s28, s28, s17\n"
    "buffer_load_dwordx4 v114, s[60:63], s28 offen lds\n"
    "v_mfma_f32_32x32x16_bf16 v[128:143], v[168:171], v[88:91], v[128:143]\n"
    "ds_read_b128 v[108:111], v120 offset:24576\n"
    "ds_read_b64_tr_b16 v[188:189], v10 offset:2048\n"
    "ds_read_b64_tr_b16 v[190:191], v10 offset:2560\n"
    "v_mfma_f32_32x32x16_bf16 v[144:159], v[200:203], v[88:91], v[144:159]\n"
    "ds_read_b64_tr_b16 v[220:221], v11 offset:2048\n"
    "ds_read_b64_tr_b16 v[222:223], v11 offset:2560\n"
    "s_add_u32 m0, s22, 33792\n"
    "s_nop 0\n"
    "buffer_load_dwordx4 v115, s[60:63], s28 offen lds\n"
    "v_mfma_f32_32x32x16_bf16 v[128:143], v[172:175], v[92:95], v[128:143]\n"
    "v_add_u32 v8, 131072, v121\n"
    "v_add_u32 v10, 131072, v122\n"
    "v_xor_b32 v9, 64, v8\n"
    "v_xor_b32 v11, 64, v10\n"
    "v_mfma_f32_32x32x16_bf16 v[144:159], v[204:207], v[92:95], v[144:159]\n"
    "s_add_u32 m0, s22, 34816\n"
    "s_nop 0\n"
    "buffer_load_dwordx4 v116, s[60:63], s28 offen lds\n"
    "s_waitcnt vmcnt(5) lgkmcnt(0)\n"
    "s_barrier\n"
    "v_smfmac_f32_16x16x64_bf16 v[224:227], v[232:235], v[176:183], v124\n"
    "s_add_u32 s28, s25, 6\n"
    "s_and_b32 s28, s28, 7\n"
    "s_lshl_b32 s28, s28, 7\n"
    "s_add_u32 m0, s10, 114688\n"
    "s_add_u32 s28, s28, s15\n"
    "buffer_load_dwordx4 v112, s[40:43], s28 offen lds\n"
    "ds_read_b128 v[80:83], v117 offset:32768\n"
    "ds_read_b64_tr_b16 v[160:161], v8 offset:0\n"
    "ds_read_b64_tr_b16 v[162:163], v8 offset:512\n"
    "v_mfma_f32_32x32x16_bf16 v[128:143], v[176:179], v[96:99], v[128:143]\n"
    "ds_read_b64_tr_b16 v[192:193], v9 offset:0\n"
    "ds_read_b64_tr_b16 v[194:195], v9 offset:512\n"
    "ds_read_b128 v[84:87], v118 offset:32768\n"
    "v_smfmac_f32_16x16x64_bf16 v[228:231], v[232:235], v[208:215], v124\n"
    "ds_read_b64_tr_b16 v[164:165], v8 offset:2048\n"
    "ds_read_b64_tr_b16 v[166:167], v8 offset:2560\n"
    "ds_read_b64_tr_b16 v[196:197], v9 offset:2048\n"
    "v_mfma_f32_32x32x16_bf16 v[144:159], v[208:211], v[96:99], v[144:159]\n"
    "s_add_u32 s29, s25, 6\n"
    "s_and_b32 s29, s29, 7\n"
    "s_lshl_b32 s28, s29, 16\n"
    "s_add_u32 m0, s10, 90112\n"
    "s_add_u32 s28, s28, s16\n"
    "buffer_load_dwordx4 v253, s[44:47], s28 offen lds\n"
    "v_mfma_f32_32x32x16_bf16 v[128:143], v[180:183], v[100:103], v[128:143]\n"
    "ds_read_b64_tr_b16 v[198:199], v9 offset:2560\n"
    "ds_read_b128 v[88:91], v119 offset:32768\n"
    "ds_read_b64_tr_b16 v[168:169], v10 offset:0\n"
    "v_mfma_f32_32x32x16_bf16 v[144:159], v[212:215], v[100:103], v[144:159]\n"
    "ds_read_b64_tr_b16 v[170:171], v10 offset:512\n"
    "ds_read_b64_tr_b16 v[200:201], v11 offset:0\n"
    "ds_read_b64_tr_b16 v[202:203], v11 offset:512\n"
    "s_mul_i32 s28, s29, 176128\n"
    "s_add_u32 m0, s22, 65536\n"
    "s_add_u32 s28, s28, s17\n"
    "buffer_load_dwordx4 v114, s[60:63], s28 offen lds\n"
    "v_mfma_f32_32x32x16_bf16 v[128:143], v[184:187], v[104:107], v[128:143]\n"
    "ds_read_b128 v[92:95], v120 offset:32768\n"
    "ds_read_b64_tr_b16 v[172:173], v10 offset:2048\n"
    "ds_read_b64_tr_b16 v[174:175], v10 offset:2560\n"
    "v_mfma_f32_32x32x16_bf16 v[144:159], v[216:219], v[104:107], v[144:159]\n"
    "ds_read_b64_tr_b16 v[204:205], v11 offset:2048\n"
    "ds_read_b64_tr_b16 v[206:207], v11 offset:2560\n"
    "s_add_u32 m0, s22, 66560\n"
    "s_nop 0\n"
    "buffer_load_dwordx4 v115, s[60:63], s28 offen lds\n"
    "v_mfma_f32_32x32x16_bf16 v[128:143], v[188:191], v[108:111], v[128:143]\n"
    "v_add_u32 v8, 32768, v121\n"
    "v_add_u32 v10, 32768, v122\n"
    "v_xor_b32 v9, 64, v8\n"
    "v_xor_b32 v11, 64, v10\n"
    "v_mfma_f32_32x32x16_bf16 v[144:159], v[220:223], v[108:111], v[144:159]\n"
    "s_add_u32 m0, s22, 67584\n"
    "s_nop 0\n"
    "buffer_load_dwordx4 v116, s[60:63], s28 offen lds\n"
    "s_waitcnt vmcnt(5) lgkmcnt(0)\n"
    "s_barrier\n"
    "v_smfmac_f32_16x16x64_bf16 v[224:227], v[232:235], v[160:167], v124\n"
    "s_add_u32 s28, s25, 7\n"
    "s_and_b32 s28, s28, 7\n"
    "s_lshl_b32 s28, s28, 7\n"
    "s_add_u32 m0, s10, 122880\n"
    "s_add_u32 s28, s28, s15\n"
    "buffer_load_dwordx4 v112, s[40:43], s28 offen lds\n"
    "v_lshlrev_b32 v0, 2, v254\n"
    "v_add_u32 v0, 158720, v0\n"
    "v_mov_b32 v1, 0\n"
    "ds_write_b32 v0, v1\n"
    "ds_read_b128 v[96:99], v117 offset:40960\n"
    "ds_read_b64_tr_b16 v[176:177], v8 offset:0\n"
    "ds_read_b64_tr_b16 v[178:179], v8 offset:512\n"
    "v_mfma_f32_32x32x16_bf16 v[128:143], v[160:163], v[80:83], v[128:143]\n"
    "ds_read_b64_tr_b16 v[208:209], v9 offset:0\n"
    "ds_read_b64_tr_b16 v[210:211], v9 offset:512\n"
    "ds_read_b128 v[100:103], v118 offset:40960\n"
    "v_smfmac_f32_16x16x64_bf16 v[228:231], v[232:235], v[192:199], v124\n"
    "ds_read_b64_tr_b16 v[180:181], v8 offset:2048\n"
    "ds_read_b64_tr_b16 v[182:183], v8 offset:2560\n"
    "ds_read_b64_tr_b16 v[212:213], v9 offset:2048\n"
    "v_mfma_f32_32x32x16_bf16 v[144:159], v[192:195], v[80:83], v[144:159]\n"
    "s_add_u32 s29, s25, 7\n"
    "s_and_b32 s29, s29, 7\n"
    "s_lshl_b32 s28, s29, 16\n"
    "s_add_u32 m0, s10, 24576\n"
    "s_add_u32 s28, s28, s16\n"
    "buffer_load_dwordx4 v253, s[44:47], s28 offen lds\n"
    "v_mfma_f32_32x32x16_bf16 v[128:143], v[164:167], v[84:87], v[128:143]\n"
    "ds_read_b64_tr_b16 v[214:215], v9 offset:2560\n"
    "ds_read_b128 v[104:107], v119 offset:40960\n"
    "ds_read_b64_tr_b16 v[184:185], v10 offset:0\n"
    "v_mfma_f32_32x32x16_bf16 v[144:159], v[196:199], v[84:87], v[144:159]\n"
    "ds_read_b64_tr_b16 v[186:187], v10 offset:512\n"
    "ds_read_b64_tr_b16 v[216:217], v11 offset:0\n"
    "ds_read_b64_tr_b16 v[218:219], v11 offset:512\n"
    "s_mul_i32 s28, s29, 176128\n"
    "s_add_u32 m0, s22, 0\n"
    "s_add_u32 s28, s28, s17\n"
    "buffer_load_dwordx4 v114, s[60:63], s28 offen lds\n"
    "v_mfma_f32_32x32x16_bf16 v[128:143], v[168:171], v[88:91], v[128:143]\n"
    "ds_read_b128 v[108:111], v120 offset:40960\n"
    "ds_read_b64_tr_b16 v[188:189], v10 offset:2048\n"
    "ds_read_b64_tr_b16 v[190:191], v10 offset:2560\n"
    "v_mfma_f32_32x32x16_bf16 v[144:159], v[200:203], v[88:91], v[144:159]\n"
    "ds_read_b64_tr_b16 v[220:221], v11 offset:2048\n"
    "ds_read_b64_tr_b16 v[222:223], v11 offset:2560\n"
    "s_add_u32 m0, s22, 1024\n"
    "s_nop 0\n"
    "buffer_load_dwordx4 v115, s[60:63], s28 offen lds\n"
    "v_mfma_f32_32x32x16_bf16 v[128:143], v[172:175], v[92:95], v[128:143]\n"
    "v_add_u32 v8, 65536, v121\n"
    "v_add_u32 v10, 65536, v122\n"
    "v_xor_b32 v9, 64, v8\n"
    "v_xor_b32 v11, 64, v10\n"
    "v_mfma_f32_32x32x16_bf16 v[144:159], v[204:207], v[92:95], v[144:159]\n"
    "s_add_u32 m0, s22, 2048\n"
    "s_nop 0\n"
    "buffer_load_dwordx4 v116, s[60:63], s28 offen lds\n"
    "s_waitcnt vmcnt(5) lgkmcnt(0)\n"
    "s_barrier\n"
    "v_smfmac_f32_16x16x64_bf16 v[224:227], v[232:235], v[176:183], v124\n"
    "ds_read_b128 v[80:83], v117 offset:49152\n"
    "ds_read_b64_tr_b16 v[160:161], v8 offset:0\n"
    "ds_read_b64_tr_b16 v[162:163], v8 offset:512\n"
    "v_mfma_f32_32x32x16_bf16 v[128:143], v[176:179], v[96:99], v[128:143]\n"
    "ds_read_b64_tr_b16 v[192:193], v9 offset:0\n"
    "ds_read_b64_tr_b16 v[194:195], v9 offset:512\n"
    "ds_read_b128 v[84:87], v118 offset:49152\n"
    "v_smfmac_f32_16x16x64_bf16 v[228:231], v[232:235], v[208:215], v124\n"
    "ds_read_b64_tr_b16 v[164:165], v8 offset:2048\n"
    "ds_read_b64_tr_b16 v[166:167], v8 offset:2560\n"
    "ds_read_b64_tr_b16 v[196:197], v9 offset:2048\n"
    "v_mfma_f32_32x32x16_bf16 v[144:159], v[208:211], v[96:99], v[144:159]\n"
    "v_mfma_f32_32x32x16_bf16 v[128:143], v[180:183], v[100:103], v[128:143]\n"
    "ds_read_b64_tr_b16 v[198:199], v9 offset:2560\n"
    "ds_read_b128 v[88:91], v119 offset:49152\n"
    "ds_read_b64_tr_b16 v[168:169], v10 offset:0\n"
    "v_mfma_f32_32x32x16_bf16 v[144:159], v[212:215], v[100:103], v[144:159]\n"
    "ds_read_b64_tr_b16 v[170:171], v10 offset:512\n"
    "ds_read_b64_tr_b16 v[200:201], v11 offset:0\n"
    "ds_read_b64_tr_b16 v[202:203], v11 offset:512\n"
    "v_mfma_f32_32x32x16_bf16 v[128:143], v[184:187], v[104:107], v[128:143]\n"
    "ds_read_b128 v[92:95], v120 offset:49152\n"
    "ds_read_b64_tr_b16 v[172:173], v10 offset:2048\n"
    "ds_read_b64_tr_b16 v[174:175], v10 offset:2560\n"
    "v_mfma_f32_32x32x16_bf16 v[144:159], v[216:219], v[104:107], v[144:159]\n"
    "ds_read_b64_tr_b16 v[204:205], v11 offset:2048\n"
    "ds_read_b64_tr_b16 v[206:207], v11 offset:2560\n"
    "v_mfma_f32_32x32x16_bf16 v[128:143], v[188:191], v[108:111], v[128:143]\n"
    "v_add_u32 v8, 0, v121\n"
    "v_add_u32 v10, 0, v122\n"
    "v_xor_b32 v9, 64, v8\n"
    "v_xor_b32 v11, 64, v10\n"
    "v_mfma_f32_32x32x16_bf16 v[144:159], v[220:223], v[108:111], v[144:159]\n"
    "s_waitcnt vmcnt(0) lgkmcnt(0)\n"
    "s_barrier\n"
    "v_smfmac_f32_16x16x64_bf16 v[224:227], v[232:235], v[160:167], v124\n"
    "ds_read_b128 v[96:99], v117 offset:57344\n"
    "ds_read_b64_tr_b16 v[176:177], v8 offset:0\n"
    "ds_read_b64_tr_b16 v[178:179], v8 offset:512\n"
    "v_mfma_f32_32x32x16_bf16 v[128:143], v[160:163], v[80:83], v[128:143]\n"
    "ds_read_b64_tr_b16 v[208:209], v9 offset:0\n"
    "ds_read_b64_tr_b16 v[210:211], v9 offset:512\n"
    "ds_read_b128 v[100:103], v118 offset:57344\n"
    "v_smfmac_f32_16x16x64_bf16 v[228:231], v[232:235], v[192:199], v124\n"
    "ds_read_b64_tr_b16 v[180:181], v8 offset:2048\n"
    "ds_read_b64_tr_b16 v[182:183], v8 offset:2560\n"
    "ds_read_b64_tr_b16 v[212:213], v9 offset:2048\n"
    "v_mfma_f32_32x32x16_bf16 v[144:159], v[192:195], v[80:83], v[144:159]\n"
    "v_mfma_f32_32x32x16_bf16 v[128:143], v[164:167], v[84:87], v[128:143]\n"
    "ds_read_b64_tr_b16 v[214:215], v9 offset:2560\n"
    "ds_read_b128 v[104:107], v119 offset:57344\n"
    "ds_read_b64_tr_b16 v[184:185], v10 offset:0\n"
    "v_mfma_f32_32x32x16_bf16 v[144:159], v[196:199], v[84:87], v[144:159]\n"
    "ds_read_b64_tr_b16 v[186:187], v10 offset:512\n"
    "ds_read_b64_tr_b16 v[216:217], v11 offset:0\n"
    "ds_read_b64_tr_b16 v[218:219], v11 offset:512\n"
    "v_mfma_f32_32x32x16_bf16 v[128:143], v[168:171], v[88:91], v[128:143]\n"
    "ds_read_b128 v[108:111], v120 offset:57344\n"
    "ds_read_b64_tr_b16 v[188:189], v10 offset:2048\n"
    "ds_read_b64_tr_b16 v[190:191], v10 offset:2560\n"
    "v_mfma_f32_32x32x16_bf16 v[144:159], v[200:203], v[88:91], v[144:159]\n"
    "ds_read_b64_tr_b16 v[220:221], v11 offset:2048\n"
    "ds_read_b64_tr_b16 v[222:223], v11 offset:2560\n"
    "v_mfma_f32_32x32x16_bf16 v[128:143], v[172:175], v[92:95], v[128:143]\n"
    "v_add_f32 v0, v224, v225\n"
    "v_add_f32 v1, v226, v227\n"
    "v_add_f32 v2, v228, v229\n"
    "v_add_f32 v3, v230, v231\n"
    "v_permlane16_swap_b32 v0, v1\n"
    "s_nop 0\n"
    "v_permlane16_swap_b32 v2, v3\n"
    "s_mov_b32 exec_hi, 0\n"
    "ds_write2_b32 v123, v0, v2 offset1:32\n"
    "s_mov_b32 exec_hi, -1\n"
    "v_xor_b32 v123, 0x100, v123\n"
    "v_mfma_f32_32x32x16_bf16 v[144:159], v[204:207], v[92:95], v[144:159]\n"
    "pq8_%=:\n"
    "s_waitcnt lgkmcnt(0)\n"
    "s_barrier\n"
    "s_lshl_b32 s21, s87, 10\n"
    "s_add_u32 s21, s21, 65536\n"
    "s_mov_b32 s88, 1\n"
    "buffer_load_dwordx4 v[4:7], v255, s[76:79], s21 offen sc1\n"
    "s_add_u32 s21, s10, 0\n"
    "s_add_u32 s23, s21, s89\n"
    "s_and_b32 s23, s23, 0x1ffff\n"
    "s_add_u32 m0, s21, 32768\n"
    "s_or_b32 s23, s23, 131072\n"
    "buffer_load_dwordx4 v255, s[76:79], s23 offen sc1 lds\n"
    "s_add_u32 s21, s10, 8192\n"
    "s_add_u32 s23, s21, s89\n"
    "s_and_b32 s23, s23, 0x1ffff\n"
    "s_add_u32 m0, s21, 32768\n"
    "s_or_b32 s23, s23, 131072\n"
    "buffer_load_dwordx4 v255, s[76:79], s23 offen sc1 lds\n"
    "s_add_u32 s21, s10, 16384\n"
    "s_add_u32 s23, s21, s89\n"
    "s_and_b32 s23, s23, 0x1ffff\n"
    "s_add_u32 m0, s21, 32768\n"
    "s_or_b32 s23, s23, 131072\n"
    "buffer_load_dwordx4 v255, s[76:79], s23 offen sc1 lds\n"
    "s_add_u32 s21, s10, 24576\n"
    "s_add_u32 s23, s21, s89\n"
    "s_and_b32 s23, s23, 0x1ffff\n"
    "s_add_u32 m0, s21, 32768\n"
    "s_or_b32 s23, s23, 131072\n"
    "buffer_load_dwordx4 v255, s[76:79], s23 offen sc1 lds\n"
    "ds_read2_b32 v[2:3], v123 offset1:32\n"
    "v_smfmac_f32_16x16x64_bf16 v[224:227], v[232:235], v[176:183], v124\n"
    "v_smfmac_f32_16x16x64_bf16 v[228:231], v[232:235], v[208:215], v124\n"
    "v_smfmac_f32_16x16x64_bf16 v[224:227], v[232:235], v[184:191], v124\n"
    "v_smfmac_f32_16x16x64_bf16 v[228:231], v[232:235], v[216:223], v124\n"
    "v_mfma_f32_32x32x16_bf16 v[128:143], v[176:179], v[96:99], v[128:143]\n"
    "v_mfma_f32_32x32x16_bf16 v[144:159], v[208:211], v[96:99], v[144:159]\n"
    "s_waitcnt lgkmcnt(0)\n"
    "s_nop 3\n"
    "v_add_f32 v224, v224, v225\n"
    "v_add_f32 v225, v226, v227\n"
    "s_nop 1\n"
    "v_permlane16_swap_b32 v224, v225\n"
    "v_mfma_f32_32x32x16_bf16 v[128:143], v[180:183], v[100:103], v[128:143]\n"
    "v_add_f32 v224, v224, v2\n"
    "v_mov_b32 v15, 0\n"
    "v_cvt_pk_bf16_f32 v12, v224, v224\n"
    "v_lshlrev_b32 v225, 16, v12\n"
    "v_mfma_f32_32x32x16_bf16 v[144:159], v[212:215], v[100:103], v[144:159]\n"
    "v_sub_f32 v224, v224, v225\n"
    "v_mov_b32 v14, 0\n"
    "v_cvt_pk_bf16_f32 v13, v224, v15\n"
    "v_add_f32 v228, v228, v229\n"
    "v_mfma_f32_32x32x16_bf16 v[128:143], v[184:187], v[104:107], v[128:143]\n"
    "v_add_f32 v229, v230, v231\n"
    "s_nop 1\n"
    "v_permlane16_swap_b32 v228, v229\n"
    "v_add_f32 v228, v228, v3\n"
    "v_mov_b32 v19, 0\n"
    "v_mfma_f32_32x32x16_bf16 v[144:159], v[216:219], v[104:107], v[144:159]\n"
    "v_cvt_pk_bf16_f32 v16, v228, v228\n"
    "v_lshlrev_b32 v229, 16, v16\n"
    "v_sub_f32 v228, v228, v229\n"
    "v_mov_b32 v18, 0\n"
    "v_mfma_f32_32x32x16_bf16 v[128:143], v[188:191], v[108:111], v[128:143]\n"
    "v_cvt_pk_bf16_f32 v17, v228, v19\n"
    "v_mfma_f32_32x32x16_bf16 v[144:159], v[220:223], v[108:111], v[144:159]\n"
    "s_waitcnt vmcnt(4)\n"
    "v_mul_f32 v160, 0x3eb8aa3b, v7\n"
    "v_mov_b32 v7, 0\n"
    "s_nop 1\n"
    "v_mfma_f32_32x32x16_bf16 v[128:143], v[12:15], v[4:7], v[128:143]\n"
    "v_mfma_f32_32x32x16_bf16 v[144:159], v[16:19], v[4:7], v[144:159]\n"
    "v_mov_b32 v204, 0x3f80\n"
    "v_mov_b32 v205, 0\n"
    "v_mov_b32 v206, 0\n"
    "v_mov_b32 v207, 0\n"
    "v_mov_b32 v209, 0\n"
    "v_mov_b32 v210, 0\n"
    "v_mov_b32 v211, 0\n"
    "v_mov_b32 v213, 0\n"
    "v_mov_b32 v214, 0\n"
    "v_mov_b32 v215, 0\n"
    "s_lshl_b32 s21, s11, 11\n"
    "s_add_u32 s21, s21, 149504\n"
    "v_add_u32 v0, s21, v255\n"
    "v_pk_mul_f32 v[128:129], v[128:129], v[160:161] op_sel:[0,0] op_sel_hi:[1,0]\n"
    "v_cvt_pk_bf16_f32 v176, v128, v129\n"
    "v_pk_mul_f32 v[130:131], v[130:131], v[160:161] op_sel:[0,0] op_sel_hi:[1,0]\n"
    "v_cvt_pk_bf16_f32 v177, v130, v131\n"
    "v_pk_mul_f32 v[132:133], v[132:133], v[160:161] op_sel:[0,0] op_sel_hi:[1,0]\n"
    "v_cvt_pk_bf16_f32 v178, v132, v133\n"
    "v_pk_mul_f32 v[134:135], v[134:135], v[160:161] op_sel:[0,0] op_sel_hi:[1,0]\n"
    "v_cvt_pk_bf16_f32 v179, v134, v135\n"
    "ds_write_b128 v0, v[176:179] offset:0\n"
    "v_pk_mul_f32 v[136:137], v[136:137], v[160:161] op_sel:[0,0] op_sel_hi:[1,0]\n"
    "v_cvt_pk_bf16_f32 v180, v136, v137\n"
    "v_pk_mul_f32 v[138:139], v[138:139], v[160:161] op_sel:[0,0] op_sel_hi:[1,0]\n"
    "v_cvt_pk_bf16_f32 v181, v138, v139\n"
    "v_pk_mul_f32 v[140:141], v[140:141], v[160:161] op_sel:[0,0] op_sel_hi:[1,0]\n"
    "v_cvt_pk_bf16_f32 v182, v140, v141\n"
    "v_pk_mul_f32 v[142:143], v[142:143], v[160:161] op_sel:[0,0] op_sel_hi:[1,0]\n"
    "v_cvt_pk_bf16_f32 v183, v142, v143\n"
    "ds_write_b128 v0, v[180:183] offset:1024\n"
    "v_pk_mul_f32 v[144:145], v[144:145], v[160:161] op_sel:[0,0] op_sel_hi:[1,0]\n"
    "v_cvt_pk_bf16_f32 v184, v144, v145\n"
    "v_pk_mul_f32 v[146:147], v[146:147], v[160:161] op_sel:[0,0] op_sel_hi:[1,0]\n"
    "v_cvt_pk_bf16_f32 v185, v146, v147\n"
    "v_pk_mul_f32 v[148:149], v[148:149], v[160:161] op_sel:[0,0] op_sel_hi:[1,0]\n"
    "v_cvt_pk_bf16_f32 v186, v148, v149\n"
    "v_pk_mul_f32 v[150:151], v[150:151], v[160:161] op_sel:[0,0] op_sel_hi:[1,0]\n"
    "v_cvt_pk_bf16_f32 v187, v150, v151\n"
    "ds_write_b128 v0, v[184:187] offset:4096\n"
    "v_pk_mul_f32 v[152:153], v[152:153], v[160:161] op_sel:[0,0] op_sel_hi:[1,0]\n"
    "v_cvt_pk_bf16_f32 v188, v152, v153\n"
    "v_pk_mul_f32 v[154:155], v[154:155], v[160:161] op_sel:[0,0] op_sel_hi:[1,0]\n"
    "v_cvt_pk_bf16_f32 v189, v154, v155\n"
    "v_pk_mul_f32 v[156:157], v[156:157], v[160:161] op_sel:[0,0] op_sel_hi:[1,0]\n"
    "v_cvt_pk_bf16_f32 v190, v156, v157\n"
    "v_pk_mul_f32 v[158:159], v[158:159], v[160:161] op_sel:[0,0] op_sel_hi:[1,0]\n"
    "v_cvt_pk_bf16_f32 v191, v158, v159\n"
    "ds_write_b128 v0, v[188:191] offset:5120\n"
    "s_waitcnt vmcnt(0) lgkmcnt(0)\n"
    "s_barrier\n"
    "ds_read_b128 v[192:195], v255 offset:32768\n"
    "ds_read_b128 v[80:83], v78\n"
    "ds_read_b128 v[84:87], v79\n"
    "ds_read_b128 v[88:91], v77 offset:32768\n"
    "ds_read_b128 v[92:95], v77 offset:34816\n"
    "s_mov_b64 exec, s[30:31]\n"
    "ds_read_b128 v[64:67], v77 offset:33792\n"
    "ds_read_b128 v[68:71], v77 offset:34048\n"
    "s_mov_b64 exec, -1\n"
    "s_waitcnt lgkmcnt(5)\n"
    "v_mfma_f32_32x32x16_bf16 v[96:111], v[192:195], v[80:83], 0\n"
    "s_waitcnt lgkmcnt(4)\n"
    "v_mfma_f32_32x32x16_bf16 v[112:127], v[192:195], v[84:87], 0\n"
    "s_nop 9\n"
    "v_max3_f32 v78, v96, v97, v98\n"
    "v_max3_f32 v78, v78, v99, v100\n"
    "v_max3_f32 v78, v78, v101, v102\n"
    "v_max3_f32 v78, v78, v103, v104\n"
    "v_max3_f32 v78, v78, v105, v106\n"
    "v_max3_f32 v78, v78, v107, v108\n"
    "v_max3_f32 v78, v78, v109, v110\n"
    "v_max_f32 v78, v78, v111\n"
    "v_max3_f32 v79, v112, v113, v114\n"
    "v_max3_f32 v79, v79, v115, v116\n"
    "v_max3_f32 v79, v79, v117, v118\n"
    "v_max3_f32 v79, v79, v119, v120\n"
    "v_max3_f32 v79, v79, v121, v122\n"
    "v_max3_f32 v79, v79, v123, v124\n"
    "v_max3_f32 v79, v79, v125, v126\n"
    "v_max_f32 v79, v79, v127\n"
    "v_mov_b32 v192, v78\n"
    "s_nop 1\n"
    "v_permlane32_swap_b32 v78, v192\n"
    "v_max_f32 v78, v78, v192\n"
    "v_mov_b32 v193, v79\n"
    "s_nop 1\n"
    "v_permlane32_swap_b32 v79, v193\n"
    "v_max_f32 v79, v79, v193\n"
    "v_mov_b32 v192, 0xc2340000\n"
    "v_fma_f32 v78, v78, -0.5, v192\n"
    "v_fma_f32 v79, v79, -0.5, v192\n"
    "v_cvt_pk_bf16_f32 v208, v78, v205\n"
    "v_cvt_pk_bf16_f32 v212, v79, v205\n"
    "v_mov_b32 v192, -1\n"
    "v_mov_b32 v193, -1\n"
    "v_mov_b32 v194, -1\n"
    "v_mov_b32 v195, -1\n"
    "v_mfma_f32_32x32x16_bf16 v[96:111], v[204:207], v[208:211], 0\n"
    "v_mfma_f32_32x32x16_bf16 v[112:127], v[204:207], v[212:215], 0\n"
    "s_waitcnt lgkmcnt(0)\n"
    "v_mfma_f32_32x32x16_bf16 v[0:15], v[88:91], v[80:83], v[96:111]\n"
    "s_branch at7_%=\n"
    "kv2_%=:\n"
    "s_and_b32 s18, s12, 1\n"
    "s_lshl_b32 s18, s18, 10\n"
    "s_lshl1_add_u32 s91, s36, s11\n"
    "s_lshl_b32 s91, s91, 11\n"
    "s_add_u32 s91, s91, s18\n"
    "s_lshl_b32 s21, s11, 2\n"
    "v_lshrrev_b32 v1, 1, v254\n"
    "v_xor_b32 v1, s21, v1\n"
    "v_and_b32 v3, 1, v254\n"
    "v_lshlrev_b32 v3, 4, v3\n"
    "v_lshl_or_b32 v113, v1, 5, v3\n"
    "s_mov_b32 s94, 0x4000\n"
    "s_mov_b32 s95, 0x20000\n"
    "s_cmp_eq_u32 s11, 0\n"
    "s_cselect_b64 s[92:93], s[48:49], s[52:53]\n"
    "s_add_u32 s28, s25, 0\n"
    "s_and_b32 s28, s28, 7\n"
    "s_lshl_b32 s28, s28, 7\n"
    "s_add_u32 m0, s10, 65536\n"
    "s_add_u32 s28, s28, s15\n"
    "buffer_load_dwordx4 v112, s[40:43], s28 offen lds\n"
    "s_add_u32 s28, s25, 1\n"
    "s_and_b32 s28, s28, 7\n"
    "s_lshl_b32 s28, s28, 7\n"
    "s_add_u32 m0, s10, 73728\n"
    "s_add_u32 s28, s28, s15\n"
    "buffer_load_dwordx4 v112, s[40:43], s28 offen lds\n"
    "s_add_u32 s28, s25, s36\n"
    "s_add_u32 s28, s28, 0\n"
    "s_and_b32 s28, s28, 7\n"
    "s_lshl_b32 s28, s28, 11\n"
    "s_add_u32 m0, s91, 32768\n"
    "s_add_u32 s28, s28, s18\n"
    "buffer_load_dwordx4 v113, s[92:95], s28 offen lds\n"
    "s_add_u32 s28, s25, 2\n"
    "s_and_b32 s28, s28, 7\n"
    "s_lshl_b32 s28, s28, 7\n"
    "s_add_u32 m0, s10, 81920\n"
    "s_add_u32 s28, s28, s15\n"
    "buffer_load_dwordx4 v112, s[40:43], s28 offen lds\n"
    "s_add_u32 s28, s25, 3\n"
    "s_and_b32 s28, s28, 7\n"
    "s_lshl_b32 s28, s28, 7\n"
    "s_add_u32 m0, s10, 90112\n"
    "s_add_u32 s28, s28, s15\n"
    "buffer_load_dwordx4 v112, s[40:43], s28 offen lds\n"
    "s_add_u32 s28, s25, s36\n"
    "s_add_u32 s28, s28, 2\n"
    "s_and_b32 s28, s28, 7\n"
    "s_lshl_b32 s28, s28, 11\n"
    "s_add_u32 m0, s91, 40960\n"
    "s_add_u32 s28, s28, s18\n"
    "buffer_load_dwordx4 v113, s[92:95], s28 offen lds\n"
    "s_load_dwordx2 s[8:9], s[76:77], 0x0\n"
    "v_lshlrev_b32 v255, 4, v254\n"
    "s_lshl1_add_u32 s87, s5, s11\n"
    "v_and_b32 v1, 31, v254\n"
    "v_lshrrev_b32 v2, 5, v254\n"
    "v_bfe_u32 v3, v254, 2, 2\n"
    "v_and_b32 v4, 3, v254\n"
    "v_lshlrev_b32 v4, 3, v4\n"
    "v_and_b32 v5, 16, v254\n"
    "v_lshl_or_b32 v7, v5, 1, v4\n"
    "v_and_b32 v8, 2, v3\n"
    "v_lshlrev_b32 v8, 5, v8\n"
    "v_lshl_or_b32 v6, v2, 3, v3\n"
    "v_bfe_u32 v9, v254, 1, 3\n"
    "v_xor_b32 v9, v9, v2\n"
    "v_lshl_or_b32 v10, s11, 5, v1\n"
    "v_lshlrev_b32 v10, 7, v10\n"
    "v_lshl_or_b32 v9, v9, 4, v10\n"
    "v_add_u32 v117, 65536, v9\n"
    "v_xor_b32 v118, 32, v117\n"
    "v_xor_b32 v119, 64, v117\n"
    "v_xor_b32 v120, 0x60, v117\n"
    "v_lshrrev_b32 v9, 2, v5\n"
    "v_xor_b32 v9, v9, v6\n"
    "v_lshl_or_b32 v9, v9, 5, v4\n"
    "v_lshl_or_b32 v9, v5, 7, v9\n"
    "v_add_u32 v125, 32768, v9\n"
    "v_xor_b32 v126, 0x80, v125\n"
    "s_lshl_b32 s39, s12, 1\n"
    "s_sub_u32 s39, s39, 2\n"
    "s_lshl_b32 s21, s39, 6\n"
    "v_or_b32 v8, v8, v7\n"
    "v_mul_u32_u24 v9, 384, v6\n"
    "v_add3_u32 v9, v9, v8, s21\n"
    "v_lshl_or_b32 v10, v6, 7, v8\n"
    "v_add_u32 v10, 0x6000, v10\n"
    "v_subrev_u32 v9, 0x6000, v10\n"
    "s_cmp_eq_u32 s12, 0\n"
    "s_cselect_b64 s[26:27], -1, 0\n"
    "v_cndmask_b32 v121, v9, v10, s[26:27]\n"
    "v_lshl_or_b32 v9, v2, 2, v3\n"
    "v_lshl_or_b32 v9, v9, 7, v8\n"
    "s_lshl_b32 s21, s12, 5\n"
    "v_xor_b32 v9, s21, v9\n"
    "s_lshl_b32 s21, s12, 11\n"
    "v_add_u32 v236, s21, v9\n"
    "v_xor_b32 v237, 64, v236\n"
    "v_add_u32 v238, 0xc000, v236\n"
    "v_add_u32 v239, 0xc000, v237\n"
    "v_add_u32 v248, 0x1c000, v236\n"
    "v_add_u32 v249, 0x1c000, v237\n"
    "v_lshlrev_b32 v242, 3, v2\n"
    "v_lshl_or_b32 v240, v1, 10, v242\n"
    "v_add_u32 v241, 0x2000, v253\n"
    "v_and_b32 v9, 15, v254\n"
    "v_cmp_gt_u32 vcc, 2, v9\n"
    "v_mov_b32 v10, 0x3f803f80\n"
    "v_and_b32 v1, 14, v9\n"
    "v_cndmask_b32 v232, 0, v10, vcc\n"
    "v_cmp_eq_u32 vcc, 2, v1\n"
    "v_mov_b32 v233, v232\n"
    "v_and_b32 v9, 1, v9\n"
    "v_cndmask_b32 v234, 0, v10, vcc\n"
    "v_mul_u32_u24 v9, 0xaaaa, v9\n"
    "v_mov_b32 v235, v234\n"
    "v_add_u32 v124, 0x4444, v9\n"
    "v_mov_b32 v224, 0\n"
    "v_mov_b32 v225, 0\n"
    "v_mov_b32 v226, 0\n"
    "v_mov_b32 v227, 0\n"
    "v_mov_b32 v228, 0\n"
    "v_mov_b32 v229, 0\n"
    "v_mov_b32 v230, 0\n"
    "v_mov_b32 v231, 0\n"
    "s_cmp_eq_u32 s12, 0\n"
    "s_cselect_b32 s21, 1, 1\n"
    "s_lshl_b32 s21, s21, 12\n"
    "s_mul_i32 s23, s21, s11\n"
    "s_sub_u32 s21, s21, s23\n"
    "v_add_u32 v122, s21, v121\n"
    "v_add_u32 v121, s23, v121\n"
    "s_lshl_b32 s21, s4, 8\n"
    "s_add_u32 s21, s21, 159744\n"
    "v_and_b32 v1, 31, v254\n"
    "v_lshl_add_u32 v123, v1, 2, s21\n"
    "v_add_u32 v8, 0, v121\n"
    "v_add_u32 v10, 0, v122\n"
    "v_xor_b32 v9, 64, v8\n"
    "v_xor_b32 v11, 64, v10\n"
    "v_lshrrev_b32 v1, 3, v254\n"
    "v_and_b32 v2, 0x7f, v253\n"
    "v_mad_u32_u24 v114, v1, s24, v2\n"
    "s_mul_i32 s22, s4, 22016\n"
    "s_add_u32 s22, s22, s17\n"
    "s_add_u32 s28, s25, 4\n"
    "s_and_b32 s28, s28, 7\n"
    "s_lshl_b32 s28, s28, 7\n"
    "s_add_u32 m0, s10, 98304\n"
    "s_add_u32 s28, s28, s15\n"
    "buffer_load_dwordx4 v112, s[40:43], s28 offen lds\n"
    "s_add_u32 s28, s25, 5\n"
    "s_and_b32 s28, s28, 7\n"
    "s_lshl_b32 s28, s28, 7\n"
    "s_add_u32 m0, s10, 106496\n"
    "s_add_u32 s28, s28, s15\n"
    "buffer_load_dwordx4 v112, s[40:43], s28 offen lds\n"
    "s_add_u32 s28, s25, s36\n"
    "s_add_u32 s28, s28, 4\n"
    "s_and_b32 s28, s28, 7\n"
    "s_lshl_b32 s28, s28, 11\n"
    "s_add_u32 m0, s91, 49152\n"
    "s_add_u32 s28, s28, s18\n"
    "buffer_load_dwordx4 v113, s[92:95], s28 offen lds\n"
    "s_add_u32 s28, s25, 6\n"
    "s_and_b32 s28, s28, 7\n"
    "s_lshl_b32 s28, s28, 7\n"
    "s_add_u32 m0, s10, 114688\n"
    "s_add_u32 s28, s28, s15\n"
    "buffer_load_dwordx4 v112, s[40:43], s28 offen lds\n"
    "s_add_u32 s28, s25, 7\n"
    "s_and_b32 s28, s28, 7\n"
    "s_lshl_b32 s28, s28, 7\n"
    "s_add_u32 m0, s10, 122880\n"
    "s_add_u32 s28, s28, s15\n"
    "buffer_load_dwordx4 v112, s[40:43], s28 offen lds\n"
    "s_add_u32 s28, s25, s36\n"
    "s_add_u32 s28, s28, 6\n"
    "s_and_b32 s28, s28, 7\n"
    "s_lshl_b32 s28, s28, 11\n"
    "s_add_u32 m0, s91, 57344\n"
    "s_add_u32 s28, s28, s18\n"
    "buffer_load_dwordx4 v113, s[92:95], s28 offen lds\n"
    "s_cmp_eq_u32 s12, 3\n"
    "s_cbranch_scc1 r14_%=\n"
    "s_cmp_eq_u32 s12, 2\n"
    "s_cbranch_scc1 r25_%=\n"
    "s_waitcnt vmcnt(6) lgkmcnt(0)\n"
    "s_barrier\n"
    "s_lshl_b32 s21, s11, 6\n"
    "v_xor_b32 v117, s21, v117\n"
    "v_xor_b32 v118, s21, v118\n"
    "v_xor_b32 v119, s21, v119\n"
    "v_xor_b32 v120, s21, v120\n"
    "ds_read_b128 v[80:83], v117\n"
    "ds_read_b128 v[84:87], v118\n"
    "ds_read_b128 v[88:91], v119\n"
    "ds_read_b128 v[92:95], v120\n"
    "s_add_u32 s29, s25, 0\n"
    "s_and_b32 s29, s29, 7\n"
    "s_lshl_b32 s28, s29, 16\n"
    "s_add_u32 m0, s10, 24576\n"
    "s_add_u32 s28, s28, s16\n"
    "buffer_load_dwordx4 v253, s[44:47], s28 offen lds\n"
    "s_mul_i32 s28, s29, 176128\n"
    "s_add_u32 m0, s10, 0\n"
    "s_add_u32 s28, s28, s22\n"
    "buffer_load_dwordx4 v114, s[60:63], s28 offen lds\n"
    "s_add_u32 s29, s25, 0\n"
    "s_and_b32 s29, s29, 7\n"
    "s_lshl_b32 s28, s29, 16\n"
    "s_add_u32 m0, s10, 28672\n"
    "s_add_u32 s28, s28, s16\n"
    "s_add_u32 s28, s28, 0x8000\n"
    "buffer_load_dwordx4 v253, s[44:47], s28 offen lds\n"
    "s_mul_i32 s28, s29, 176128\n"
    "s_add_u32 m0, s10, 4096\n"
    "s_add_u32 s28, s28, s22\n"
    "s_add_u32 s28, s28, 88064\n"
    "buffer_load_dwordx4 v114, s[60:63], s28 offen lds\n"
    "s_add_u32 s29, s25, 1\n"
    "s_and_b32 s29, s29, 7\n"
    "s_lshl_b32 s28, s29, 16\n"
    "s_add_u32 m0, s10, 155648\n"
    "s_add_u32 s28, s28, s16\n"
    "buffer_load_dwordx4 v253, s[44:47], s28 offen lds\n"
    "s_mul_i32 s28, s29, 176128\n"
    "s_add_u32 m0, s10, 131072\n"
    "s_add_u32 s28, s28, s22\n"
    "buffer_load_dwordx4 v114, s[60:63], s28 offen lds\n"
    "s_add_u32 s29, s25, 1\n"
    "s_and_b32 s29, s29, 7\n"
    "s_lshl_b32 s28, s29, 16\n"
    "s_add_u32 m0, s10, 159744\n"
    "s_add_u32 s28, s28, s16\n"
    "s_add_u32 s28, s28, 0x8000\n"
    "buffer_load_dwordx4 v253, s[44:47], s28 offen lds\n"
    "s_mul_i32 s28, s29, 176128\n"
    "s_add_u32 m0, s10, 135168\n"
    "s_add_u32 s28, s28, s22\n"
    "s_add_u32 s28, s28, 88064\n"
    "buffer_load_dwordx4 v114, s[60:63], s28 offen lds\n"
    "s_waitcnt vmcnt(8) lgkmcnt(0)\n"
    "s_barrier\n"
    "v_and_b32 v1, 27, v254\n"
    "v_cmp_eq_u32 vcc, 16, v1\n"
    "v_mov_b32 v1, 0x3f803f80\n"
    "v_mov_b32 v76, 0xdddd\n"
    "v_cndmask_b32 v64, 0, v1, vcc\n"
    "v_mov_b32 v65, v64\n"
    "v_mov_b32 v66, v64\n"
    "v_mov_b32 v67, v64\n"
    "v_mov_b32 v68, v64\n"
    "v_mov_b32 v69, v64\n"
    "v_mov_b32 v70, v64\n"
    "v_mov_b32 v71, v64\n"
    "v_mov_b32 v72, v64\n"
    "v_mov_b32 v73, v64\n"
    "v_mov_b32 v74, v64\n"
    "v_mov_b32 v75, v64\n"
    "v_mov_b32 v24, 0\n"
    "v_mov_b32 v25, 0\n"
    "v_mov_b32 v26, 0\n"
    "v_mov_b32 v27, 0\n"
    "v_mov_b32 v28, 0\n"
    "v_mov_b32 v29, 0\n"
    "v_mov_b32 v30, 0\n"
    "v_mov_b32 v31, 0\n"
    "v_mov_b32 v32, 0\n"
    "v_mov_b32 v33, 0\n"
    "v_mov_b32 v34, 0\n"
    "v_mov_b32 v35, 0\n"
    "v_mov_b32 v36, 0\n"
    "v_mov_b32 v37, 0\n"
    "v_mov_b32 v38, 0\n"
    "v_mov_b32 v39, 0\n"
    "v_mov_b32 v40, 0\n"
    "v_mov_b32 v41, 0\n"
    "v_mov_b32 v42, 0\n"
    "v_mov_b32 v43, 0\n"
    "v_mov_b32 v44, 0\n"
    "v_mov_b32 v45, 0\n"
    "v_mov_b32 v46, 0\n"
    "v_mov_b32 v47, 0\n"
    "v_mov_b32 v48, 0\n"
    "v_mov_b32 v49, 0\n"
    "v_mov_b32 v50, 0\n"
    "v_mov_b32 v51, 0\n"
    "v_mov_b32 v52, 0\n"
    "v_mov_b32 v53, 0\n"
    "v_mov_b32 v54, 0\n"
    "v_mov_b32 v55, 0\n"
    "v_mov_b32 v56, 0\n"
    "v_mov_b32 v57, 0\n"
    "v_mov_b32 v58, 0\n"
    "v_mov_b32 v59, 0\n"
    "v_mov_b32 v60, 0\n"
    "v_mov_b32 v61, 0\n"
    "v_mov_b32 v62, 0\n"
    "v_mov_b32 v63, 0\n"
    "s_mul_i32 s19, s87, 122880\n"
    "s_add_u32 s19, s19, 262144\n"
    "s_mov_b32 s88, 0\n"
    "s_mov_b32 s14, 0x7f000000\n"
    "s_lshr_b32 s89, s2, 3\n"
    "s_lshl_b32 s89, s89, 12\n"
    "s_lshl_b32 s21, s36, 14\n"
    "v_add_u32 v77, s21, v255\n"
    "v_add_u32 v250, 0x10000, v77\n"
    "s_and_b32 s21, s4, 3\n"
    "s_lshl1_add_u32 s21, s21, s36\n"
    "s_lshl_b32 s21, s21, 10\n"
    "s_add_u32 s21, s21, 149504\n"
    "v_add_u32 v78, s21, v255\n"
    "v_xor_b32 v79, 0x400, v78\n"
    "s_mov_b32 s30, 0xffff\n"
    "s_mov_b32 s31, 0xffff\n"
    "s_lshl_b32 s35, s5, 9\n"
    "s_add_u32 s35, s35, 16384\n"
    "s_lshl3_add_u32 s21, s6, s4\n"
    "s_lshl3_add_u32 s34, s21, s35\n"
    "s_mul_i32 s86, s12, 22528\n"
    "s_add_u32 s86, s86, s19\n"
    "s_and_b32 s21, s4, 2\n"
    "s_add_u32 s21, s21, s36\n"
    "s_lshl2_add_u32 s21, s6, s21\n"
    "s_add_u32 s21, s21, 88\n"
    "s_lshl_b32 s21, s21, 10\n"
    "s_add_u32 s20, s21, s19\n"
    "s_mul_i32 s21, s4, 2304\n"
    "s_add_u32 s21, s21, 131072\n"
    "v_add_u32 v243, s21, v255\n"
    "v_lshl_add_u32 v244, v254, 2, s21\n"
    "s_xor_b32 s21, s4, 4\n"
    "s_mul_i32 s21, s21, 2304\n"
    "s_add_u32 s21, s21, 131072\n"
    "v_add_u32 v245, s21, v255\n"
    "v_lshl_add_u32 v246, v254, 2, s21\n"
    "v_and_b32 v247, 3, v254\n"
    "v_lshlrev_b32 v247, 2, v247\n"
    "v_add_u32 v247, 158720, v247\n"
    "v_lshlrev_b32 v127, 7, v254\n"
    "v_and_b32 v1, 31, v254\n"
    "v_lshlrev_b32 v1, 1, v1\n"
    "s_lshl_b32 s21, s39, 6\n"
    "s_add_u32 s21, s21, s17\n"
    "buffer_load_ushort v22, v1, s[64:67], s21 offen\n"
    "buffer_load_ushort v23, v1, s[64:67], s21 offen offset:64\n"
    "s_waitcnt vmcnt(6) lgkmcnt(0)\n"
    "s_barrier\n"
    "s_add_u32 s8, s8, 1\n"
    "s_addc_u32 s9, s9, 0\n"
    "ds_read_b64_tr_b16 v[160:161], v8 offset:0\n"
    "ds_read_b64_tr_b16 v[162:163], v8 offset:512\n"
    "ds_read_b64_tr_b16 v[192:193], v9 offset:0\n"
    "ds_read_b64_tr_b16 v[194:195], v9 offset:512\n"
    "ds_read_b64_tr_b16 v[164:165], v8 offset:2048\n"
    "ds_read_b64_tr_b16 v[166:167], v8 offset:2560\n"
    "ds_read_b64_tr_b16 v[196:197], v9 offset:2048\n"
    "ds_read_b64_tr_b16 v[198:199], v9 offset:2560\n"
    "ds_read_b64_tr_b16 v[168:169], v10 offset:0\n"
    "s_add_u32 s29, s25, 2\n"
    "s_and_b32 s29, s29, 7\n"
    "s_lshl_b32 s28, s29, 16\n"
    "s_add_u32 m0, s10, 57344\n"
    "s_add_u32 s28, s28, s16\n"
    "buffer_load_dwordx4 v253, s[44:47], s28 offen lds\n"
    "ds_read_b64_tr_b16 v[170:171], v10 offset:512\n"
    "ds_read_b64_tr_b16 v[200:201], v11 offset:0\n"
    "ds_read_b64_tr_b16 v[202:203], v11 offset:512\n"
    "ds_read_b64_tr_b16 v[172:173], v10 offset:2048\n"
    "ds_read_b64_tr_b16 v[174:175], v10 offset:2560\n"
    "ds_read_b64_tr_b16 v[204:205], v11 offset:2048\n"
    "s_mul_i32 s28, s29, 176128\n"
    "s_add_u32 m0, s10, 32768\n"
    "s_add_u32 s28, s28, s22\n"
    "buffer_load_dwordx4 v114, s[60:63], s28 offen lds\n"
    "ds_read_b64_tr_b16 v[206:207], v11 offset:2560\n"
    "v_add_u32 v8, 131072, v121\n"
    "v_add_u32 v10, 131072, v122\n"
    "v_xor_b32 v9, 64, v8\n"
    "v_xor_b32 v11, 64, v10\n"
    "s_waitcnt vmcnt(4) lgkmcnt(0)\n"
    "s_barrier\n"
    "v_smfmac_f32_16x16x64_bf16 v[224:227], v[232:235], v[160:167], v124\n"
    "ds_read_b128 v[96:99], v117 offset:8192\n"
    "ds_read_b64_tr_b16 v[176:177], v8 offset:0\n"
    "ds_read_b64_tr_b16 v[178:179], v8 offset:512\n"
    "v_mfma_f32_32x32x16_bf16 v[128:143], v[160:163], v[80:83], 0\n"
    "ds_read_b64_tr_b16 v[208:209], v9 offset:0\n"
    "ds_read_b64_tr_b16 v[210:211], v9 offset:512\n"
    "ds_read_b128 v[100:103], v118 offset:8192\n"
    "v_smfmac_f32_16x16x64_bf16 v[228:231], v[232:235], v[192:199], v124\n"
    "ds_read_b64_tr_b16 v[180:181], v8 offset:2048\n"
    "ds_read_b64_tr_b16 v[182:183], v8 offset:2560\n"
    "ds_read_b64_tr_b16 v[212:213], v9 offset:2048\n"
    "v_mfma_f32_32x32x16_bf16 v[144:159], v[192:195], v[80:83], 0\n"
    "s_add_u32 s29, s25, 3\n"
    "s_and_b32 s29, s29, 7\n"
    "s_lshl_b32 s28, s29, 16\n"
    "s_add_u32 m0, s10, 24576\n"
    "s_add_u32 s28, s28, s16\n"
    "buffer_load_dwordx4 v253, s[44:47], s28 offen lds\n"
    "v_mfma_f32_32x32x16_bf16 v[128:143], v[164:167], v[84:87], v[128:143]\n"
    "ds_read_b64_tr_b16 v[214:215], v9 offset:2560\n"
    "ds_read_b128 v[104:107], v119 offset:8192\n"
    "ds_read_b64_tr_b16 v[184:185], v10 offset:0\n"
    "v_mfma_f32_32x32x16_bf16 v[144:159], v[196:199], v[84:87], v[144:159]\n"
    "ds_read_b64_tr_b16 v[186:187], v10 offset:512\n"
    "ds_read_b64_tr_b16 v[216:217], v11 offset:0\n"
    "ds_read_b64_tr_b16 v[218:219], v11 offset:512\n"
    "s_mul_i32 s28, s29, 176128\n"
    "s_add_u32 m0, s10, 0\n"
    "s_add_u32 s28, s28, s22\n"
    "buffer_load_dwordx4 v114, s[60:63], s28 offen lds\n"
    "v_mfma_f32_32x32x16_bf16 v[128:143], v[168:171], v[88:91], v[128:143]\n"
    "ds_read_b128 v[108:111], v120 offset:8192\n"
    "ds_read_b64_tr_b16 v[188:189], v10 offset:2048\n"
    "ds_read_b64_tr_b16 v[190:191], v10 offset:2560\n"
    "v_mfma_f32_32x32x16_bf16 v[144:159], v[200:203], v[88:91], v[144:159]\n"
    "ds_read_b64_tr_b16 v[220:221], v11 offset:2048\n"
    "ds_read_b64_tr_b16 v[222:223], v11 offset:2560\n"
    "v_mfma_f32_32x32x16_bf16 v[128:143], v[172:175], v[92:95], v[128:143]\n"
    "v_add_u32 v8, 32768, v121\n"
    "v_add_u32 v10, 32768, v122\n"
    "v_xor_b32 v9, 64, v8\n"
    "v_xor_b32 v11, 64, v10\n"
    "v_mfma_f32_32x32x16_bf16 v[144:159], v[204:207], v[92:95], v[144:159]\n"
    "s_waitcnt vmcnt(2) lgkmcnt(0)\n"
    "s_barrier\n"
    "v_smfmac_f32_16x16x64_bf16 v[224:227], v[232:235], v[176:183], v124\n"
    "ds_read_b128 v[80:83], v117 offset:16384\n"
    "ds_read_b64_tr_b16 v[160:161], v8 offset:0\n"
    "ds_read_b64_tr_b16 v[162:163], v8 offset:512\n"
    "v_mfma_f32_32x32x16_bf16 v[128:143], v[176:179], v[96:99], v[128:143]\n"
    "ds_read_b64_tr_b16 v[192:193], v9 offset:0\n"
    "ds_read_b64_tr_b16 v[194:195], v9 offset:512\n"
    "ds_read_b128 v[84:87], v118 offset:16384\n"
    "v_smfmac_f32_16x16x64_bf16 v[228:231], v[232:235], v[208:215], v124\n"
    "ds_read_b64_tr_b16 v[164:165], v8 offset:2048\n"
    "ds_read_b64_tr_b16 v[166:167], v8 offset:2560\n"
    "ds_read_b64_tr_b16 v[196:197], v9 offset:2048\n"
    "v_mfma_f32_32x32x16_bf16 v[144:159], v[208:211], v[96:99], v[144:159]\n"
    "s_add_u32 s29, s25, 4\n"
    "s_and_b32 s29, s29, 7\n"
    "s_lshl_b32 s28, s29, 16\n"
    "s_add_u32 m0, s10, 155648\n"
    "s_add_u32 s28, s28, s16\n"
    "buffer_load_dwordx4 v253, s[44:47], s28 offen lds\n"
    "v_mfma_f32_32x32x16_bf16 v[128:143], v[180:183], v[100:103], v[128:143]\n"
    "ds_read_b64_tr_b16 v[198:199], v9 offset:2560\n"
    "ds_read_b128 v[88:91], v119 offset:16384\n"
    "ds_read_b64_tr_b16 v[168:169], v10 offset:0\n"
    "v_mfma_f32_32x32x16_bf16 v[144:159], v[212:215], v[100:103], v[144:159]\n"
    "ds_read_b64_tr_b16 v[170:171], v10 offset:512\n"
    "ds_read_b64_tr_b16 v[200:201], v11 offset:0\n"
    "ds_read_b64_tr_b16 v[202:203], v11 offset:512\n"
    "s_mul_i32 s28, s29, 176128\n"
    "s_add_u32 m0, s10, 131072\n"
    "s_add_u32 s28, s28, s22\n"
    "buffer_load_dwordx4 v114, s[60:63], s28 offen lds\n"
    "v_mfma_f32_32x32x16_bf16 v[128:143], v[184:187], v[104:107], v[128:143]\n"
    "ds_read_b128 v[92:95], v120 offset:16384\n"
    "ds_read_b64_tr_b16 v[172:173], v10 offset:2048\n"
    "ds_read_b64_tr_b16 v[174:175], v10 offset:2560\n"
    "v_mfma_f32_32x32x16_bf16 v[144:159], v[216:219], v[104:107], v[144:159]\n"
    "ds_read_b64_tr_b16 v[204:205], v11 offset:2048\n"
    "ds_read_b64_tr_b16 v[206:207], v11 offset:2560\n"
    "v_mfma_f32_32x32x16_bf16 v[128:143], v[188:191], v[108:111], v[128:143]\n"
    "v_add_u32 v8, 0, v121\n"
    "v_add_u32 v10, 0, v122\n"
    "v_xor_b32 v9, 64, v8\n"
    "v_xor_b32 v11, 64, v10\n"
    "v_mfma_f32_32x32x16_bf16 v[144:159], v[220:223], v[108:111], v[144:159]\n"
    "s_waitcnt vmcnt(2) lgkmcnt(0)\n"
    "s_barrier\n"
    "v_smfmac_f32_16x16x64_bf16 v[224:227], v[232:235], v[160:167], v124\n"
    "ds_read_b128 v[96:99], v117 offset:24576\n"
    "ds_read_b64_tr_b16 v[176:177], v8 offset:0\n"
    "ds_read_b64_tr_b16 v[178:179], v8 offset:512\n"
    "v_mfma_f32_32x32x16_bf16 v[128:143], v[160:163], v[80:83], v[128:143]\n"
    "ds_read_b64_tr_b16 v[208:209], v9 offset:0\n"
    "ds_read_b64_tr_b16 v[210:211], v9 offset:512\n"
    "ds_read_b128 v[100:103], v118 offset:24576\n"
    "v_smfmac_f32_16x16x64_bf16 v[228:231], v[232:235], v[192:199], v124\n"
    "ds_read_b64_tr_b16 v[180:181], v8 offset:2048\n"
    "ds_read_b64_tr_b16 v[182:183], v8 offset:2560\n"
    "ds_read_b64_tr_b16 v[212:213], v9 offset:2048\n"
    "v_mfma_f32_32x32x16_bf16 v[144:159], v[192:195], v[80:83], v[144:159]\n"
    "s_add_u32 s29, s25, 5\n"
    "s_and_b32 s29, s29, 7\n"
    "s_lshl_b32 s28, s29, 16\n"
    "s_add_u32 m0, s10, 57344\n"
    "s_add_u32 s28, s28, s16\n"
    "buffer_load_dwordx4 v253, s[44:47], s28 offen lds\n"
    "v_mfma_f32_32x32x16_bf16 v[128:143], v[164:167], v[84:87], v[128:143]\n"
    "ds_read_b64_tr_b16 v[214:215], v9 offset:2560\n"
    "ds_read_b128 v[104:107], v119 offset:24576\n"
    "ds_read_b64_tr_b16 v[184:185], v10 offset:0\n"
    "v_mfma_f32_32x32x16_bf16 v[144:159], v[196:199], v[84:87], v[144:159]\n"
    "ds_read_b64_tr_b16 v[186:187], v10 offset:512\n"
    "ds_read_b64_tr_b16 v[216:217], v11 offset:0\n"
    "ds_read_b64_tr_b16 v[218:219], v11 offset:512\n"
    "s_mul_i32 s28, s29, 176128\n"
    "s_add_u32 m0, s10, 32768\n"
    "s_add_u32 s28, s28, s22\n"
    "buffer_load_dwordx4 v114, s[60:63], s28 offen lds\n"
    "v_mfma_f32_32x32x16_bf16 v[128:143], v[168:171], v[88:91], v[128:143]\n"
    "ds_read_b128 v[108:111], v120 offset:24576\n"
    "ds_read_b64_tr_b16 v[188:189], v10 offset:2048\n"
    "ds_read_b64_tr_b16 v[190:191], v10 offset:2560\n"
    "v_mfma_f32_32x32x16_bf16 v[144:159], v[200:203], v[88:91], v[144:159]\n"
    "ds_read_b64_tr_b16 v[220:221], v11 offset:2048\n"
    "ds_read_b64_tr_b16 v[222:223], v11 offset:2560\n"
    "v_mfma_f32_32x32x16_bf16 v[128:143], v[172:175], v[92:95], v[128:143]\n"
    "v_add_u32 v8, 131072, v121\n"
    "v_add_u32 v10, 131072, v122\n"
    "v_xor_b32 v9, 64, v8\n"
    "v_xor_b32 v11, 64, v10\n"
    "v_mfma_f32_32x32x16_bf16 v[144:159], v[204:207], v[92:95], v[144:159]\n"
    "s_waitcnt vmcnt(2) lgkmcnt(0)\n"
    "s_barrier\n"
    "v_smfmac_f32_16x16x64_bf16 v[224:227], v[232:235], v[176:183], v124\n"
    "ds_read_b128 v[80:83], v117 offset:32768\n"
    "ds_read_b64_tr_b16 v[160:161], v8 offset:0\n"
    "ds_read_b64_tr_b16 v[162:163], v8 offset:512\n"
    "v_mfma_f32_32x32x16_bf16 v[128:143], v[176:179], v[96:99], v[128:143]\n"
    "ds_read_b64_tr_b16 v[192:193], v9 offset:0\n"
    "ds_read_b64_tr_b16 v[194:195], v9 offset:512\n"
    "ds_read_b128 v[84:87], v118 offset:32768\n"
    "v_smfmac_f32_16x16x64_bf16 v[228:231], v[232:235], v[208:215], v124\n"
    "ds_read_b64_tr_b16 v[164:165], v8 offset:2048\n"
    "ds_read_b64_tr_b16 v[166:167], v8 offset:2560\n"
    "ds_read_b64_tr_b16 v[196:197], v9 offset:2048\n"
    "v_mfma_f32_32x32x16_bf16 v[144:159], v[208:211], v[96:99], v[144:159]\n"
    "s_add_u32 s29, s25, 6\n"
    "s_and_b32 s29, s29, 7\n"
    "s_lshl_b32 s28, s29, 16\n"
    "s_add_u32 m0, s10, 90112\n"
    "s_add_u32 s28, s28, s16\n"
    "buffer_load_dwordx4 v253, s[44:47], s28 offen lds\n"
    "v_mfma_f32_32x32x16_bf16 v[128:143], v[180:183], v[100:103], v[128:143]\n"
    "ds_read_b64_tr_b16 v[198:199], v9 offset:2560\n"
    "ds_read_b128 v[88:91], v119 offset:32768\n"
    "ds_read_b64_tr_b16 v[168:169], v10 offset:0\n"
    "v_mfma_f32_32x32x16_bf16 v[144:159], v[212:215], v[100:103], v[144:159]\n"
    "ds_read_b64_tr_b16 v[170:171], v10 offset:512\n"
    "ds_read_b64_tr_b16 v[200:201], v11 offset:0\n"
    "ds_read_b64_tr_b16 v[202:203], v11 offset:512\n"
    "s_mul_i32 s28, s29, 176128\n"
    "s_add_u32 m0, s10, 65536\n"
    "s_add_u32 s28, s28, s22\n"
    "buffer_load_dwordx4 v114, s[60:63], s28 offen lds\n"
    "v_mfma_f32_32x32x16_bf16 v[128:143], v[184:187], v[104:107], v[128:143]\n"
    "ds_read_b128 v[92:95], v120 offset:32768\n"
    "ds_read_b64_tr_b16 v[172:173], v10 offset:2048\n"
    "ds_read_b64_tr_b16 v[174:175], v10 offset:2560\n"
    "v_mfma_f32_32x32x16_bf16 v[144:159], v[216:219], v[104:107], v[144:159]\n"
    "ds_read_b64_tr_b16 v[204:205], v11 offset:2048\n"
    "ds_read_b64_tr_b16 v[206:207], v11 offset:2560\n"
    "v_mfma_f32_32x32x16_bf16 v[128:143], v[188:191], v[108:111], v[128:143]\n"
    "v_add_u32 v8, 32768, v121\n"
    "v_add_u32 v10, 32768, v122\n"
    "v_xor_b32 v9, 64, v8\n"
    "v_xor_b32 v11, 64, v10\n"
    "v_mfma_f32_32x32x16_bf16 v[144:159], v[220:223], v[108:111], v[144:159]\n"
    "s_waitcnt vmcnt(2) lgkmcnt(0)\n"
    "s_barrier\n"
    "v_smfmac_f32_16x16x64_bf16 v[224:227], v[232:235], v[160:167], v124\n"
    "v_lshlrev_b32 v0, 2, v254\n"
    "v_add_u32 v0, 158720, v0\n"
    "v_mov_b32 v1, 0\n"
    "ds_write_b32 v0, v1\n"
    "ds_read_b128 v[96:99], v117 offset:40960\n"
    "ds_read_b64_tr_b16 v[176:177], v8 offset:0\n"
    "ds_read_b64_tr_b16 v[178:179], v8 offset:512\n"
    "v_mfma_f32_32x32x16_bf16 v[128:143], v[160:163], v[80:83], v[128:143]\n"
    "ds_read_b64_tr_b16 v[208:209], v9 offset:0\n"
    "ds_read_b64_tr_b16 v[210:211], v9 offset:512\n"
    "ds_read_b128 v[100:103], v118 offset:40960\n"
    "v_smfmac_f32_16x16x64_bf16 v[228:231], v[232:235], v[192:199], v124\n"
    "ds_read_b64_tr_b16 v[180:181], v8 offset:2048\n"
    "ds_read_b64_tr_b16 v[182:183], v8 offset:2560\n"
    "ds_read_b64_tr_b16 v[212:213], v9 offset:2048\n"
    "v_mfma_f32_32x32x16_bf16 v[144:159], v[192:195], v[80:83], v[144:159]\n"
    "s_add_u32 s29, s25, 7\n"
    "s_and_b32 s29, s29, 7\n"
    "s_lshl_b32 s28, s29, 16\n"
    "s_add_u32 m0, s10, 24576\n"
    "s_add_u32 s28, s28, s16\n"
    "buffer_load_dwordx4 v253, s[44:47], s28 offen lds\n"
    "v_mfma_f32_32x32x16_bf16 v[128:143], v[164:167], v[84:87], v[128:143]\n"
    "ds_read_b64_tr_b16 v[214:215], v9 offset:2560\n"
    "ds_read_b128 v[104:107], v119 offset:40960\n"
    "ds_read_b64_tr_b16 v[184:185], v10 offset:0\n"
    "v_mfma_f32_32x32x16_bf16 v[144:159], v[196:199], v[84:87], v[144:159]\n"
    "ds_read_b64_tr_b16 v[186:187], v10 offset:512\n"
    "ds_read_b64_tr_b16 v[216:217], v11 offset:0\n"
    "ds_read_b64_tr_b16 v[218:219], v11 offset:512\n"
    "s_mul_i32 s28, s29, 176128\n"
    "s_add_u32 m0, s10, 0\n"
    "s_add_u32 s28, s28, s22\n"
    "buffer_load_dwordx4 v114, s[60:63], s28 offen lds\n"
    "v_mfma_f32_32x32x16_bf16 v[128:143], v[168:171], v[88:91], v[128:143]\n"
    "ds_read_b128 v[108:111], v120 offset:40960\n"
    "ds_read_b64_tr_b16 v[188:189], v10 offset:2048\n"
    "ds_read_b64_tr_b16 v[190:191], v10 offset:2560\n"
    "v_mfma_f32_32x32x16_bf16 v[144:159], v[200:203], v[88:91], v[144:159]\n"
    "ds_read_b64_tr_b16 v[220:221], v11 offset:2048\n"
    "ds_read_b64_tr_b16 v[222:223], v11 offset:2560\n"
    "v_mfma_f32_32x32x16_bf16 v[128:143], v[172:175], v[92:95], v[128:143]\n"
    "v_add_u32 v8, 65536, v121\n"
    "v_add_u32 v10, 65536, v122\n"
    "v_xor_b32 v9, 64, v8\n"
    "v_xor_b32 v11, 64, v10\n"
    "v_mfma_f32_32x32x16_bf16 v[144:159], v[204:207], v[92:95], v[144:159]\n"
    "s_waitcnt vmcnt(2) lgkmcnt(0)\n"
    "s_barrier\n"
    "v_smfmac_f32_16x16x64_bf16 v[224:227], v[232:235], v[176:183], v124\n"
    "ds_read_b128 v[80:83], v117 offset:49152\n"
    "ds_read_b64_tr_b16 v[160:161], v8 offset:0\n"
    "ds_read_b64_tr_b16 v[162:163], v8 offset:512\n"
    "v_mfma_f32_32x32x16_bf16 v[128:143], v[176:179], v[96:99], v[128:143]\n"
    "ds_read_b64_tr_b16 v[192:193], v9 offset:0\n"
    "ds_read_b64_tr_b16 v[194:195], v9 offset:512\n"
    "ds_read_b128 v[84:87], v118 offset:49152\n"
    "v_smfmac_f32_16x16x64_bf16 v[228:231], v[232:235], v[208:215], v124\n"
    "ds_read_b64_tr_b16 v[164:165], v8 offset:2048\n"
    "ds_read_b64_tr_b16 v[166:167], v8 offset:2560\n"
    "ds_read_b64_tr_b16 v[196:197], v9 offset:2048\n"
    "v_mfma_f32_32x32x16_bf16 v[144:159], v[208:211], v[96:99], v[144:159]\n"
    "v_mfma_f32_32x32x16_bf16 v[128:143], v[180:183], v[100:103], v[128:143]\n"
    "ds_read_b64_tr_b16 v[198:199], v9 offset:2560\n"
    "ds_read_b128 v[88:91], v119 offset:49152\n"
    "ds_read_b64_tr_b16 v[168:169], v10 offset:0\n"
    "v_mfma_f32_32x32x16_bf16 v[144:159], v[212:215], v[100:103], v[144:159]\n"
    "ds_read_b64_tr_b16 v[170:171], v10 offset:512\n"
    "ds_read_b64_tr_b16 v[200:201], v11 offset:0\n"
    "ds_read_b64_tr_b16 v[202:203], v11 offset:512\n"
    "v_mfma_f32_32x32x16_bf16 v[128:143], v[184:187], v[104:107], v[128:143]\n"
    "ds_read_b128 v[92:95], v120 offset:49152\n"
    "ds_read_b64_tr_b16 v[172:173], v10 offset:2048\n"
    "ds_read_b64_tr_b16 v[174:175], v10 offset:2560\n"
    "v_mfma_f32_32x32x16_bf16 v[144:159], v[216:219], v[104:107], v[144:159]\n"
    "ds_read_b64_tr_b16 v[204:205], v11 offset:2048\n"
    "ds_read_b64_tr_b16 v[206:207], v11 offset:2560\n"
    "v_mfma_f32_32x32x16_bf16 v[128:143], v[188:191], v[108:111], v[128:143]\n"
    "v_add_u32 v8, 0, v121\n"
    "v_add_u32 v10, 0, v122\n"
    "v_xor_b32 v9, 64, v8\n"
    "v_xor_b32 v11, 64, v10\n"
    "v_mfma_f32_32x32x16_bf16 v[144:159], v[220:223], v[108:111], v[144:159]\n"
    "s_waitcnt vmcnt(0) lgkmcnt(0)\n"
    "s_barrier\n"
    "v_smfmac_f32_16x16x64_bf16 v[224:227], v[232:235], v[160:167], v124\n"
    "ds_read_b128 v[96:99], v117 offset:57344\n"
    "ds_read_b64_tr_b16 v[176:177], v8 offset:0\n"
    "ds_read_b64_tr_b16 v[178:179], v8 offset:512\n"
    "v_mfma_f32_32x32x16_bf16 v[128:143], v[160:163], v[80:83], v[128:143]\n"
    "ds_read_b64_tr_b16 v[208:209], v9 offset:0\n"
    "ds_read_b64_tr_b16 v[210:211], v9 offset:512\n"
    "ds_read_b128 v[100:103], v118 offset:57344\n"
    "v_smfmac_f32_16x16x64_bf16 v[228:231], v[232:235], v[192:199], v124\n"
    "ds_read_b64_tr_b16 v[180:181], v8 offset:2048\n"
    "ds_read_b64_tr_b16 v[182:183], v8 offset:2560\n"
    "ds_read_b64_tr_b16 v[212:213], v9 offset:2048\n"
    "v_mfma_f32_32x32x16_bf16 v[144:159], v[192:195], v[80:83], v[144:159]\n"
    "v_mfma_f32_32x32x16_bf16 v[128:143], v[164:167], v[84:87], v[128:143]\n"
    "ds_read_b64_tr_b16 v[214:215], v9 offset:2560\n"
    "ds_read_b128 v[104:107], v119 offset:57344\n"
    "ds_read_b64_tr_b16 v[184:185], v10 offset:0\n"
    "v_mfma_f32_32x32x16_bf16 v[144:159], v[196:199], v[84:87], v[144:159]\n"
    "ds_read_b64_tr_b16 v[186:187], v10 offset:512\n"
    "ds_read_b64_tr_b16 v[216:217], v11 offset:0\n"
    "ds_read_b64_tr_b16 v[218:219], v11 offset:512\n"
    "v_mfma_f32_32x32x16_bf16 v[128:143], v[168:171], v[88:91], v[128:143]\n"
    "ds_read_b128 v[108:111], v120 offset:57344\n"
    "ds_read_b64_tr_b16 v[188:189], v10 offset:2048\n"
    "ds_read_b64_tr_b16 v[190:191], v10 offset:2560\n"
    "v_mfma_f32_32x32x16_bf16 v[144:159], v[200:203], v[88:91], v[144:159]\n"
    "ds_read_b64_tr_b16 v[220:221], v11 offset:2048\n"
    "ds_read_b64_tr_b16 v[222:223], v11 offset:2560\n"
    "v_mfma_f32_32x32x16_bf16 v[128:143], v[172:175], v[92:95], v[128:143]\n"
    "v_add_f32 v0, v224, v225\n"
    "v_add_f32 v1, v226, v227\n"
    "v_add_f32 v2, v228, v229\n"
    "v_add_f32 v3, v230, v231\n"
    "v_permlane16_swap_b32 v0, v1\n"
    "s_nop 0\n"
    "v_permlane16_swap_b32 v2, v3\n"
    "s_mov_b32 exec_hi, 0\n"
    "ds_write2_b32 v123, v0, v2 offset1:32\n"
    "s_mov_b32 exec_hi, -1\n"
    "v_xor_b32 v123, 0x100, v123\n"
    "v_mfma_f32_32x32x16_bf16 v[144:159], v[204:207], v[92:95], v[144:159]\n"
    "s_cmp_eq_u32 s12, 0\n"
    "s_cbranch_scc1 pq8_%=\n"
    "s_branch pp9_%=\n"
    "r14_%=:\n"
    "s_waitcnt vmcnt(6) lgkmcnt(0)\n"
    "s_barrier\n"
    "s_setprio 2\n"
    "ds_read_b64_tr_b16 v[176:177], v125 offset:0\n"
    "ds_read_b64_tr_b16 v[178:179], v126 offset:0\n"
    "ds_read_b128 v[144:147], v117 offset:0\n"
    "ds_read_b64_tr_b16 v[180:181], v125 offset:512\n"
    "ds_read_b64_tr_b16 v[182:183], v126 offset:512\n"
    "ds_read_b128 v[148:151], v118 offset:0\n"
    "ds_read_b64_tr_b16 v[184:185], v125 offset:1024\n"
    "ds_read_b64_tr_b16 v[186:187], v126 offset:1024\n"
    "ds_read_b128 v[152:155], v119 offset:0\n"
    "ds_read_b64_tr_b16 v[188:189], v125 offset:1536\n"
    "ds_read_b64_tr_b16 v[190:191], v126 offset:1536\n"
    "ds_read_b128 v[156:159], v120 offset:0\n"
    "ds_read_b64_tr_b16 v[192:193], v125 offset:4096\n"
    "ds_read_b64_tr_b16 v[194:195], v126 offset:4096\n"
    "ds_read_b128 v[160:163], v117 offset:8192\n"
    "s_waitcnt lgkmcnt(9)\n"
    "v_smfmac_f32_16x16x64_bf16 v[224:227], v[232:235], v[176:183], v124\n"
    "v_mfma_f32_32x32x16_bf16 v[128:143], v[176:179], v[144:147], 0\n"
    "ds_read_b64_tr_b16 v[196:197], v125 offset:4608\n"
    "ds_read_b64_tr_b16 v[198:199], v126 offset:4608\n"
    "ds_read_b128 v[164:167], v118 offset:8192\n"
    "v_mfma_f32_32x32x16_bf16 v[128:143], v[180:183], v[148:151], v[128:143]\n"
    "ds_read_b64_tr_b16 v[200:201], v125 offset:5120\n"
    "ds_read_b64_tr_b16 v[202:203], v126 offset:5120\n"
    "ds_read_b128 v[168:171], v119 offset:8192\n"
    "s_waitcnt lgkmcnt(9)\n"
    "v_smfmac_f32_16x16x64_bf16 v[224:227], v[232:235], v[184:191], v124\n"
    "v_mfma_f32_32x32x16_bf16 v[128:143], v[184:187], v[152:155], v[128:143]\n"
    "ds_read_b64_tr_b16 v[204:205], v125 offset:5632\n"
    "ds_read_b64_tr_b16 v[206:207], v126 offset:5632\n"
    "ds_read_b128 v[172:175], v120 offset:8192\n"
    "v_mfma_f32_32x32x16_bf16 v[128:143], v[188:191], v[156:159], v[128:143]\n"
    "ds_read_b64_tr_b16 v[176:177], v125 offset:8192\n"
    "ds_read_b64_tr_b16 v[178:179], v126 offset:8192\n"
    "ds_read_b128 v[144:147], v117 offset:16384\n"
    "s_waitcnt lgkmcnt(9)\n"
    "v_smfmac_f32_16x16x64_bf16 v[224:227], v[232:235], v[192:199], v124\n"
    "v_mfma_f32_32x32x16_bf16 v[128:143], v[192:195], v[160:163], v[128:143]\n"
    "ds_read_b64_tr_b16 v[180:181], v125 offset:8704\n"
    "ds_read_b64_tr_b16 v[182:183], v126 offset:8704\n"
    "ds_read_b128 v[148:151], v118 offset:16384\n"
    "v_mfma_f32_32x32x16_bf16 v[128:143], v[196:199], v[164:167], v[128:143]\n"
    "ds_read_b64_tr_b16 v[184:185], v125 offset:9216\n"
    "ds_read_b64_tr_b16 v[186:187], v126 offset:9216\n"
    "ds_read_b128 v[152:155], v119 offset:16384\n"
    "s_waitcnt lgkmcnt(9)\n"
    "v_smfmac_f32_16x16x64_bf16 v[224:227], v[232:235], v[200:207], v124\n"
    "v_mfma_f32_32x32x16_bf16 v[128:143], v[200:203], v[168:171], v[128:143]\n"
    "ds_read_b64_tr_b16 v[188:189], v125 offset:9728\n"
    "ds_read_b64_tr_b16 v[190:191], v126 offset:9728\n"
    "ds_read_b128 v[156:159], v120 offset:16384\n"
    "v_mfma_f32_32x32x16_bf16 v[128:143], v[204:207], v[172:175], v[128:143]\n"
    "ds_read_b64_tr_b16 v[192:193], v125 offset:12288\n"
    "ds_read_b64_tr_b16 v[194:195], v126 offset:12288\n"
    "ds_read_b128 v[160:163], v117 offset:24576\n"
    "s_waitcnt lgkmcnt(9)\n"
    "v_smfmac_f32_16x16x64_bf16 v[224:227], v[232:235], v[176:183], v124\n"
    "v_mfma_f32_32x32x16_bf16 v[128:143], v[176:179], v[144:147], v[128:143]\n"
    "ds_read_b64_tr_b16 v[196:197], v125 offset:12800\n"
    "ds_read_b64_tr_b16 v[198:199], v126 offset:12800\n"
    "ds_read_b128 v[164:167], v118 offset:24576\n"
    "v_mfma_f32_32x32x16_bf16 v[128:143], v[180:183], v[148:151], v[128:143]\n"
    "ds_read_b64_tr_b16 v[200:201], v125 offset:13312\n"
    "ds_read_b64_tr_b16 v[202:203], v126 offset:13312\n"
    "ds_read_b128 v[168:171], v119 offset:24576\n"
    "s_waitcnt lgkmcnt(9)\n"
    "v_smfmac_f32_16x16x64_bf16 v[224:227], v[232:235], v[184:191], v124\n"
    "v_mfma_f32_32x32x16_bf16 v[128:143], v[184:187], v[152:155], v[128:143]\n"
    "ds_read_b64_tr_b16 v[204:205], v125 offset:13824\n"
    "ds_read_b64_tr_b16 v[206:207], v126 offset:13824\n"
    "ds_read_b128 v[172:175], v120 offset:24576\n"
    "v_mfma_f32_32x32x16_bf16 v[128:143], v[188:191], v[156:159], v[128:143]\n"
    "s_waitcnt vmcnt(0)\n"
    "s_barrier\n"
    "ds_read_b64_tr_b16 v[176:177], v125 offset:16384\n"
    "ds_read_b64_tr_b16 v[178:179], v126 offset:16384\n"
    "ds_read_b128 v[144:147], v117 offset:32768\n"
    "s_waitcnt lgkmcnt(9)\n"
    "v_smfmac_f32_16x16x64_bf16 v[224:227], v[232:235], v[192:199], v124\n"
    "v_mfma_f32_32x32x16_bf16 v[128:143], v[192:195], v[160:163], v[128:143]\n"
    "ds_read_b64_tr_b16 v[180:181], v125 offset:16896\n"
    "ds_read_b64_tr_b16 v[182:183], v126 offset:16896\n"
    "ds_read_b128 v[148:151], v118 offset:32768\n"
    "v_mfma_f32_32x32x16_bf16 v[128:143], v[196:199], v[164:167], v[128:143]\n"
    "ds_read_b64_tr_b16 v[184:185], v125 offset:17408\n"
    "ds_read_b64_tr_b16 v[186:187], v126 offset:17408\n"
    "ds_read_b128 v[152:155], v119 offset:32768\n"
    "s_waitcnt lgkmcnt(9)\n"
    "v_smfmac_f32_16x16x64_bf16 v[224:227], v[232:235], v[200:207], v124\n"
    "v_mfma_f32_32x32x16_bf16 v[128:143], v[200:203], v[168:171], v[128:143]\n"
    "ds_read_b64_tr_b16 v[188:189], v125 offset:17920\n"
    "ds_read_b64_tr_b16 v[190:191], v126 offset:17920\n"
    "ds_read_b128 v[156:159], v120 offset:32768\n"
    "v_mfma_f32_32x32x16_bf16 v[128:143], v[204:207], v[172:175], v[128:143]\n"
    "ds_read_b64_tr_b16 v[192:193], v125 offset:20480\n"
    "ds_read_b64_tr_b16 v[194:195], v126 offset:20480\n"
    "ds_read_b128 v[160:163], v117 offset:40960\n"
    "s_waitcnt lgkmcnt(9)\n"
    "v_smfmac_f32_16x16x64_bf16 v[224:227], v[232:235], v[176:183], v124\n"
    "v_mfma_f32_32x32x16_bf16 v[128:143], v[176:179], v[144:147], v[128:143]\n"
    "ds_read_b64_tr_b16 v[196:197], v125 offset:20992\n"
    "ds_read_b64_tr_b16 v[198:199], v126 offset:20992\n"
    "ds_read_b128 v[164:167], v118 offset:40960\n"
    "v_mfma_f32_32x32x16_bf16 v[128:143], v[180:183], v[148:151], v[128:143]\n"
    "ds_read_b64_tr_b16 v[200:201], v125 offset:21504\n"
    "ds_read_b64_tr_b16 v[202:203], v126 offset:21504\n"
    "ds_read_b128 v[168:171], v119 offset:40960\n"
    "s_waitcnt lgkmcnt(9)\n"
    "v_smfmac_f32_16x16x64_bf16 v[224:227], v[232:235], v[184:191], v124\n"
    "v_mfma_f32_32x32x16_bf16 v[128:143], v[184:187], v[152:155], v[128:143]\n"
    "ds_read_b64_tr_b16 v[204:205], v125 offset:22016\n"
    "ds_read_b64_tr_b16 v[206:207], v126 offset:22016\n"
    "ds_read_b128 v[172:175], v120 offset:40960\n"
    "v_mfma_f32_32x32x16_bf16 v[128:143], v[188:191], v[156:159], v[128:143]\n"
    "ds_read_b64_tr_b16 v[176:177], v125 offset:24576\n"
    "ds_read_b64_tr_b16 v[178:179], v126 offset:24576\n"
    "ds_read_b128 v[144:147], v117 offset:49152\n"
    "s_waitcnt lgkmcnt(9)\n"
    "v_smfmac_f32_16x16x64_bf16 v[224:227], v[232:235], v[192:199], v124\n"
    "v_mfma_f32_32x32x16_bf16 v[128:143], v[192:195], v[160:163], v[128:143]\n"
    "ds_read_b64_tr_b16 v[180:181], v125 offset:25088\n"
    "ds_read_b64_tr_b16 v[182:183], v126 offset:25088\n"
    "ds_read_b128 v[148:151], v118 offset:49152\n"
    "v_mfma_f32_32x32x16_bf16 v[128:143], v[196:199], v[164:167], v[128:143]\n"
    "ds_read_b64_tr_b16 v[184:185], v125 offset:25600\n"
    "ds_read_b64_tr_b16 v[186:187], v126 offset:25600\n"
    "ds_read_b128 v[152:155], v119 offset:49152\n"
    "s_waitcnt lgkmcnt(9)\n"
    "v_smfmac_f32_16x16x64_bf16 v[224:227], v[232:235], v[200:207], v124\n"
    "v_mfma_f32_32x32x16_bf16 v[128:143], v[200:203], v[168:171], v[128:143]\n"
    "ds_read_b64_tr_b16 v[188:189], v125 offset:26112\n"
    "ds_read_b64_tr_b16 v[190:191], v126 offset:26112\n"
    "ds_read_b128 v[156:159], v120 offset:49152\n"
    "v_mfma_f32_32x32x16_bf16 v[128:143], v[204:207], v[172:175], v[128:143]\n"
    "ds_read_b64_tr_b16 v[192:193], v125 offset:28672\n"
    "ds_read_b64_tr_b16 v[194:195], v126 offset:28672\n"
    "ds_read_b128 v[160:163], v117 offset:57344\n"
    "s_waitcnt lgkmcnt(9)\n"
    "v_smfmac_f32_16x16x64_bf16 v[224:227], v[232:235], v[176:183], v124\n"
    "v_mfma_f32_32x32x16_bf16 v[128:143], v[176:179], v[144:147], v[128:143]\n"
    "ds_read_b64_tr_b16 v[196:197], v125 offset:29184\n"
    "ds_read_b64_tr_b16 v[198:199], v126 offset:29184\n"
    "ds_read_b128 v[164:167], v118 offset:57344\n"
    "v_mfma_f32_32x32x16_bf16 v[128:143], v[180:183], v[148:151], v[128:143]\n"
    "ds_read_b64_tr_b16 v[200:201], v125 offset:29696\n"
    "ds_read_b64_tr_b16 v[202:203], v126 offset:29696\n"
    "ds_read_b128 v[168:171], v119 offset:57344\n"
    "s_waitcnt lgkmcnt(9)\n"
    "v_smfmac_f32_16x16x64_bf16 v[224:227], v[232:235], v[184:191], v124\n"
    "v_mfma_f32_32x32x16_bf16 v[128:143], v[184:187], v[152:155], v[128:143]\n"
    "ds_read_b64_tr_b16 v[204:205], v125 offset:30208\n"
    "ds_read_b64_tr_b16 v[206:207], v126 offset:30208\n"
    "ds_read_b128 v[172:175], v120 offset:57344\n"
    "v_mfma_f32_32x32x16_bf16 v[128:143], v[188:191], v[156:159], v[128:143]\n"
    "s_waitcnt lgkmcnt(6)\n"
    "v_smfmac_f32_16x16x64_bf16 v[224:227], v[232:235], v[192:199], v124\n"
    "v_mfma_f32_32x32x16_bf16 v[128:143], v[192:195], v[160:163], v[128:143]\n"
    "v_mfma_f32_32x32x16_bf16 v[128:143], v[196:199], v[164:167], v[128:143]\n"
    "s_waitcnt lgkmcnt(0)\n"
    "v_smfmac_f32_16x16x64_bf16 v[224:227], v[232:235], v[200:207], v124\n"
    "v_mfma_f32_32x32x16_bf16 v[128:143], v[200:203], v[168:171], v[128:143]\n"
    "v_mfma_f32_32x32x16_bf16 v[128:143], v[204:207], v[172:175], v[128:143]\n"
    "s_branch ph6_%=\n"
    "r25_%=:\n"
    "s_waitcnt vmcnt(6) lgkmcnt(0)\n"
    "s_barrier\n"
    "s_setprio 2\n"
    "ds_read_b128 v[144:147], v117 offset:0\n"
    "ds_read_b128 v[148:151], v118 offset:0\n"
    "ds_read_b128 v[152:155], v119 offset:0\n"
    "ds_read_b128 v[156:159], v120 offset:0\n"
    "ds_read_b128 v[160:163], v117 offset:8192\n"
    "ds_read_b128 v[164:167], v118 offset:8192\n"
    "s_waitcnt lgkmcnt(4)\n"
    "v_smfmac_f32_16x16x64_bf16 v[224:227], v[232:235], v[144:151], v124\n"
    "v_mfma_f32_32x32x16_bf16 v[128:143], v[144:147], v[144:147], 0\n"
    "ds_read_b128 v[168:171], v119 offset:8192\n"
    "v_mfma_f32_32x32x16_bf16 v[128:143], v[148:151], v[148:151], v[128:143]\n"
    "ds_read_b128 v[172:175], v120 offset:8192\n"
    "s_waitcnt lgkmcnt(4)\n"
    "v_smfmac_f32_16x16x64_bf16 v[224:227], v[232:235], v[152:159], v124\n"
    "v_mfma_f32_32x32x16_bf16 v[128:143], v[152:155], v[152:155], v[128:143]\n"
    "ds_read_b128 v[144:147], v117 offset:16384\n"
    "v_mfma_f32_32x32x16_bf16 v[128:143], v[156:159], v[156:159], v[128:143]\n"
    "ds_read_b128 v[148:151], v118 offset:16384\n"
    "s_waitcnt lgkmcnt(4)\n"
    "v_smfmac_f32_16x16x64_bf16 v[224:227], v[232:235], v[160:167], v124\n"
    "v_mfma_f32_32x32x16_bf16 v[128:143], v[160:163], v[160:163], v[128:143]\n"
    "ds_read_b128 v[152:155], v119 offset:16384\n"
    "v_mfma_f32_32x32x16_bf16 v[128:143], v[164:167], v[164:167], v[128:143]\n"
    "ds_read_b128 v[156:159], v120 offset:16384\n"
    "s_waitcnt lgkmcnt(4)\n"
    "v_smfmac_f32_16x16x64_bf16 v[224:227], v[232:235], v[168:175], v124\n"
    "v_mfma_f32_32x32x16_bf16 v[128:143], v[168:171], v[168:171], v[128:143]\n"
    "ds_read_b128 v[160:163], v117 offset:24576\n"
    "v_mfma_f32_32x32x16_bf16 v[128:143], v[172:175], v[172:175], v[128:143]\n"
    "ds_read_b128 v[164:167], v118 offset:24576\n"
    "s_waitcnt lgkmcnt(4)\n"
    "v_smfmac_f32_16x16x64_bf16 v[224:227], v[232:235], v[144:151], v124\n"
    "v_mfma_f32_32x32x16_bf16 v[128:143], v[144:147], v[144:147], v[128:143]\n"
    "ds_read_b128 v[168:171], v119 offset:24576\n"
    "v_mfma_f32_32x32x16_bf16 v[128:143], v[148:151], v[148:151], v[128:143]\n"
    "ds_read_b128 v[172:175], v120 offset:24576\n"
    "s_waitcnt lgkmcnt(4)\n"
    "v_smfmac_f32_16x16x64_bf16 v[224:227], v[232:235], v[152:159], v124\n"
    "v_mfma_f32_32x32x16_bf16 v[128:143], v[152:155], v[152:155], v[128:143]\n"
    "s_waitcnt vmcnt(0)\n"
    "s_barrier\n"
    "ds_read_b128 v[144:147], v117 offset:32768\n"
    "v_mfma_f32_32x32x16_bf16 v[128:143], v[156:159], v[156:159], v[128:143]\n"
    "ds_read_b128 v[148:151], v118 offset:32768\n"
    "s_waitcnt lgkmcnt(4)\n"
    "v_smfmac_f32_16x16x64_bf16 v[224:227], v[232:235], v[160:167], v124\n"
    "v_mfma_f32_32x32x16_bf16 v[128:143], v[160:163], v[160:163], v[128:143]\n"
    "ds_read_b128 v[152:155], v119 offset:32768\n"
    "v_mfma_f32_32x32x16_bf16 v[128:143], v[164:167], v[164:167], v[128:143]\n"
    "ds_read_b128 v[156:159], v120 offset:32768\n"
    "s_waitcnt lgkmcnt(4)\n"
    "v_smfmac_f32_16x16x64_bf16 v[224:227], v[232:235], v[168:175], v124\n"
    "v_mfma_f32_32x32x16_bf16 v[128:143], v[168:171], v[168:171], v[128:143]\n"
    "ds_read_b128 v[160:163], v117 offset:40960\n"
    "v_mfma_f32_32x32x16_bf16 v[128:143], v[172:175], v[172:175], v[128:143]\n"
    "ds_read_b128 v[164:167], v118 offset:40960\n"
    "s_waitcnt lgkmcnt(4)\n"
    "v_smfmac_f32_16x16x64_bf16 v[224:227], v[232:235], v[144:151], v124\n"
    "v_mfma_f32_32x32x16_bf16 v[128:143], v[144:147], v[144:147], v[128:143]\n"
    "ds_read_b128 v[168:171], v119 offset:40960\n"
    "v_mfma_f32_32x32x16_bf16 v[128:143], v[148:151], v[148:151], v[128:143]\n"
    "ds_read_b128 v[172:175], v120 offset:40960\n"
    "s_waitcnt lgkmcnt(4)\n"
    "v_smfmac_f32_16x16x64_bf16 v[224:227], v[232:235], v[152:159], v124\n"
    "v_mfma_f32_32x32x16_bf16 v[128:143], v[152:155], v[152:155], v[128:143]\n"
    "ds_read_b128 v[144:147], v117 offset:49152\n"
    "v_mfma_f32_32x32x16_bf16 v[128:143], v[156:159], v[156:159], v[128:143]\n"
    "ds_read_b128 v[148:151], v118 offset:49152\n"
    "s_waitcnt lgkmcnt(4)\n"
    "v_smfmac_f32_16x16x64_bf16 v[224:227], v[232:235], v[160:167], v124\n"
    "v_mfma_f32_32x32x16_bf16 v[128:143], v[160:163], v[160:163], v[128:143]\n"
    "ds_read_b128 v[152:155], v119 offset:49152\n"
    "v_mfma_f32_32x32x16_bf16 v[128:143], v[164:167], v[164:167], v[128:143]\n"
    "ds_read_b128 v[156:159], v120 offset:49152\n"
    "s_waitcnt lgkmcnt(4)\n"
    "v_smfmac_f32_16x16x64_bf16 v[224:227], v[232:235], v[168:175], v124\n"
    "v_mfma_f32_32x32x16_bf16 v[128:143], v[168:171], v[168:171], v[128:143]\n"
    "ds_read_b128 v[160:163], v117 offset:57344\n"
    "v_mfma_f32_32x32x16_bf16 v[128:143], v[172:175], v[172:175], v[128:143]\n"
    "ds_read_b128 v[164:167], v118 offset:57344\n"
    "s_waitcnt lgkmcnt(4)\n"
    "v_smfmac_f32_16x16x64_bf16 v[224:227], v[232:235], v[144:151], v124\n"
    "v_mfma_f32_32x32x16_bf16 v[128:143], v[144:147], v[144:147], v[128:143]\n"
    "ds_read_b128 v[168:171], v119 offset:57344\n"
    "v_mfma_f32_32x32x16_bf16 v[128:143], v[148:151], v[148:151], v[128:143]\n"
    "ds_read_b128 v[172:175], v120 offset:57344\n"
    "s_waitcnt lgkmcnt(4)\n"
    "v_smfmac_f32_16x16x64_bf16 v[224:227], v[232:235], v[152:159], v124\n"
    "v_mfma_f32_32x32x16_bf16 v[128:143], v[152:155], v[152:155], v[128:143]\n"
    "v_mfma_f32_32x32x16_bf16 v[128:143], v[156:159], v[156:159], v[128:143]\n"
    "s_waitcnt lgkmcnt(2)\n"
    "v_smfmac_f32_16x16x64_bf16 v[224:227], v[232:235], v[160:167], v124\n"
    "v_mfma_f32_32x32x16_bf16 v[128:143], v[160:163], v[160:163], v[128:143]\n"
    "v_mfma_f32_32x32x16_bf16 v[128:143], v[164:167], v[164:167], v[128:143]\n"
    "s_waitcnt lgkmcnt(0)\n"
    "v_smfmac_f32_16x16x64_bf16 v[224:227], v[232:235], v[168:175], v124\n"
    "v_mfma_f32_32x32x16_bf16 v[128:143], v[168:171], v[168:171], v[128:143]\n"
    "v_mfma_f32_32x32x16_bf16 v[128:143], v[172:175], v[172:175], v[128:143]\n"
    "v_and_b32 v209, 31, v254\n"
    "s_nop 4\n"
    "v_add_f32 v224, v224, v225\n"
    "v_add_f32 v225, v226, v227\n"
    "s_nop 1\n"
    "v_permlane16_swap_b32 v224, v225\n"
    "v_mov_b32 v210, v224\n"
    "s_nop 1\n"
    "v_permlane32_swap_b32 v210, v224\n"
    "s_mov_b32 s26, 0xaaaaaaaa\n"
    "s_mov_b32 s27, 0xaaaaaaaa\n"
    "v_cndmask_b32 v128, v128, v129, s[26:27]\n"
    "v_cndmask_b32 v129, v130, v131, s[26:27]\n"
    "v_cndmask_b32 v130, v132, v133, s[26:27]\n"
    "v_cndmask_b32 v131, v134, v135, s[26:27]\n"
    "v_cndmask_b32 v132, v136, v137, s[26:27]\n"
    "v_cndmask_b32 v133, v138, v139, s[26:27]\n"
    "v_cndmask_b32 v134, v140, v141, s[26:27]\n"
    "v_cndmask_b32 v135, v142, v143, s[26:27]\n"
    "s_mov_b32 s26, 0xcccccccc\n"
    "s_mov_b32 s27, 0xcccccccc\n"
    "v_cndmask_b32 v128, v128, v129, s[26:27]\n"
    "v_cndmask_b32 v129, v130, v131, s[26:27]\n"
    "v_cndmask_b32 v130, v132, v133, s[26:27]\n"
    "v_cndmask_b32 v131, v134, v135, s[26:27]\n"
    "s_mov_b32 s26, 0xff00ff00\n"
    "s_mov_b32 s27, 0xff00ff00\n"
    "v_cndmask_b32 v128, v128, v129, s[26:27]\n"
    "v_cndmask_b32 v129, v130, v131, s[26:27]\n"
    "s_mov_b32 s26, 0xffff0000\n"
    "s_mov_b32 s27, 0xffff0000\n"
    "v_cndmask_b32 v128, v128, v129, s[26:27]\n"
    "v_mul_f32 v210, 0xbb000000, v210\n"
    "v_mul_f32 v211, 0x3b000000, v128\n"
    "v_fma_f32 v211, -v210, v210, v211\n"
    "v_max_f32 v211, 0, v211\n"
    "v_add_f32 v211, 0x3727c5ac, v211\n"
    "v_rsq_f32 v215, v211\n"
    "v_cvt_pk_bf16_f32 v212, v210, v210\n"
    "v_mul_f32 v211, v211, v215\n"
    "v_lshlrev_b32 v212, 16, v212\n"
    "v_cvt_pk_bf16_f32 v213, v210, v211\n"
    "v_sub_f32 v212, v210, v212\n"
    "v_and_b32 v214, 0xffff0000, v213\n"
    "v_cvt_pk_bf16_f32 v212, v210, v212\n"
    "v_sub_f32 v214, v211, v214\n"
    "v_mov_b32 v216, 0\n"
    "v_cvt_pk_bf16_f32 v214, v214, v216\n"
    "v_mov_b32 v217, 0\n"
    "v_mov_b32 v218, 0\n"
    "v_mov_b32 v219, v215\n"
    "s_lshl_b32 s21, s11, 10\n"
    "s_add_u32 s21, s21, 69632\n"
    "v_lshl_add_u32 v208, v209, 4, s21\n"
    "s_mov_b32 s26, 0x0f0f0f0f\n"
    "s_mov_b32 s27, 0xf0f0f0f0\n"
    "s_mov_b64 exec, s[26:27]\n"
    "ds_write_b128 v208, v[212:215]\n"
    "ds_write_b128 v208, v[216:219] offset:512\n"
    "s_mov_b64 exec, -1\n"
    "ph6_%=:\n"
    "s_waitcnt lgkmcnt(0)\n"
    "s_barrier\n"
    "s_add_u32 s8, s8, 1\n"
    "s_addc_u32 s9, s9, 0\n"
    "s_cmp_eq_u32 s12, 3\n"
    "s_cbranch_scc0 r11_%=\n"
    "s_lshl_b32 s21, s11, 10\n"
    "s_add_u32 s21, s21, 69632\n"
    "v_add_u32 v208, s21, v255\n"
    "ds_read_b128 v[208:211], v208\n"
    "v_add_f32 v224, v224, v225\n"
    "v_add_f32 v225, v226, v227\n"
    "s_nop 1\n"
    "v_permlane16_swap_b32 v224, v225\n"
    "v_mov_b32 v219, 0\n"
    "v_cvt_pk_bf16_f32 v216, v224, v224\n"
    "v_lshlrev_b32 v225, 16, v216\n"
    "v_sub_f32 v224, v224, v225\n"
    "v_mov_b32 v218, 0\n"
    "v_cvt_pk_bf16_f32 v217, v224, v219\n"
    "s_lshl_b32 s21, s87, 10\n"
    "s_add_u32 s24, s21, 65536\n"
    "s_lshl_b32 s21, s87, 11\n"
    "s_add_u32 s23, s21, 131072\n"
    "s_waitcnt lgkmcnt(0)\n"
    "buffer_store_dwordx4 v[208:211], v255, s[76:79], s24 offen sc1\n"
    "v_mov_b32 v220, v208\n"
    "v_mov_b32 v221, v209\n"
    "v_mov_b32 v222, v210\n"
    "v_mov_b32 v223, 0\n"
    "v_mov_b32 v214, v211\n"
    "s_nop 0\n"
    "v_mfma_f32_32x32x16_bf16 v[128:143], v[216:219], v[220:223], v[128:143]\n"
    "v_and_b32 v192, 31, v254\n"
    "v_lshrrev_b32 v194, 5, v254\n"
    "v_lshlrev_b32 v193, 8, v194\n"
    "v_lshl_or_b32 v192, v192, 1, v193\n"
    "s_lshl_b32 s21, s11, 10\n"
    "s_add_u32 s21, s21, 67584\n"
    "v_add_u32 v192, s21, v192\n"
    "v_and_b32 v193, 15, v254\n"
    "v_and_b32 v195, 16, v254\n"
    "v_lshlrev_b32 v193, 6, v193\n"
    "v_lshl_or_b32 v193, v195, 1, v193\n"
    "v_lshl_or_b32 v193, v194, 4, v193\n"
    "v_add_u32 v193, s21, v193\n"
    "v_pk_mul_f32 v[136:137], v[136:137], v[214:215] op_sel:[0,0] op_sel_hi:[1,0]\n"
    "v_cvt_pk_bf16_f32 v180, v136, v137\n"
    "ds_write_b16 v192, v180 offset:0\n"
    "ds_write_b16_d16_hi v192, v180 offset:64\n"
    "v_pk_mul_f32 v[138:139], v[138:139], v[214:215] op_sel:[0,0] op_sel_hi:[1,0]\n"
    "v_cvt_pk_bf16_f32 v181, v138, v139\n"
    "ds_write_b16 v192, v181 offset:128\n"
    "ds_write_b16_d16_hi v192, v181 offset:192\n"
    "v_pk_mul_f32 v[140:141], v[140:141], v[214:215] op_sel:[0,0] op_sel_hi:[1,0]\n"
    "v_cvt_pk_bf16_f32 v182, v140, v141\n"
    "ds_write_b16 v192, v182 offset:512\n"
    "ds_write_b16_d16_hi v192, v182 offset:576\n"
    "v_pk_mul_f32 v[142:143], v[142:143], v[214:215] op_sel:[0,0] op_sel_hi:[1,0]\n"
    "v_cvt_pk_bf16_f32 v183, v142, v143\n"
    "ds_write_b16 v192, v183 offset:640\n"
    "ds_write_b16_d16_hi v192, v183 offset:704\n"
    "ds_read_b128 v[184:187], v193\n"
    "v_pk_mul_f32 v[128:129], v[128:129], v[214:215] op_sel:[0,0] op_sel_hi:[1,0]\n"
    "v_cvt_pk_bf16_f32 v176, v128, v129\n"
    "v_pk_mul_f32 v[130:131], v[130:131], v[214:215] op_sel:[0,0] op_sel_hi:[1,0]\n"
    "v_cvt_pk_bf16_f32 v177, v130, v131\n"
    "v_pk_mul_f32 v[132:133], v[132:133], v[214:215] op_sel:[0,0] op_sel_hi:[1,0]\n"
    "v_cvt_pk_bf16_f32 v178, v132, v133\n"
    "v_pk_mul_f32 v[134:135], v[134:135], v[214:215] op_sel:[0,0] op_sel_hi:[1,0]\n"
    "v_cvt_pk_bf16_f32 v179, v134, v135\n"
    "buffer_store_dwordx4 v[176:179], v255, s[76:79], s23 offen sc1\n"
    "s_waitcnt lgkmcnt(0)\n"
    "buffer_store_dwordx4 v[184:187], v255, s[76:79], s23 offen offset:1024 sc1\n"
    "r11_%=:\n"
    "s_setprio 0\n"
    "s_add_u32 s29, s25, 2\n"
    "s_and_b32 s29, s29, 7\n"
    "s_lshl_b32 s28, s29, 16\n"
    "s_add_u32 m0, s10, 57344\n"
    "s_add_u32 s28, s28, s16\n"
    "buffer_load_dwordx4 v253, s[44:47], s28 offen lds\n"
    "s_mul_i32 s28, s29, 176128\n"
    "s_add_u32 m0, s10, 32768\n"
    "s_add_u32 s28, s28, s22\n"
    "buffer_load_dwordx4 v114, s[60:63], s28 offen lds\n"
    "s_waitcnt lgkmcnt(0)\n"
    "s_barrier\n"
    "v_and_b32 v1, 27, v254\n"
    "v_cmp_eq_u32 vcc, 16, v1\n"
    "v_mov_b32 v1, 0x3f803f80\n"
    "v_mov_b32 v76, 0xdddd\n"
    "v_cndmask_b32 v64, 0, v1, vcc\n"
    "v_mov_b32 v65, v64\n"
    "v_mov_b32 v66, v64\n"
    "v_mov_b32 v67, v64\n"
    "v_mov_b32 v68, v64\n"
    "v_mov_b32 v69, v64\n"
    "v_mov_b32 v70, v64\n"
    "v_mov_b32 v71, v64\n"
    "v_mov_b32 v72, v64\n"
    "v_mov_b32 v73, v64\n"
    "v_mov_b32 v74, v64\n"
    "v_mov_b32 v75, v64\n"
    "v_mov_b32 v24, 0\n"
    "v_mov_b32 v25, 0\n"
    "v_mov_b32 v26, 0\n"
    "v_mov_b32 v27, 0\n"
    "v_mov_b32 v28, 0\n"
    "v_mov_b32 v29, 0\n"
    "v_mov_b32 v30, 0\n"
    "v_mov_b32 v31, 0\n"
    "v_mov_b32 v32, 0\n"
    "v_mov_b32 v33, 0\n"
    "v_mov_b32 v34, 0\n"
    "v_mov_b32 v35, 0\n"
    "v_mov_b32 v36, 0\n"
    "v_mov_b32 v37, 0\n"
    "v_mov_b32 v38, 0\n"
    "v_mov_b32 v39, 0\n"
    "v_mov_b32 v40, 0\n"
    "v_mov_b32 v41, 0\n"
    "v_mov_b32 v42, 0\n"
    "v_mov_b32 v43, 0\n"
    "v_mov_b32 v44, 0\n"
    "v_mov_b32 v45, 0\n"
    "v_mov_b32 v46, 0\n"
    "v_mov_b32 v47, 0\n"
    "v_mov_b32 v48, 0\n"
    "v_mov_b32 v49, 0\n"
    "v_mov_b32 v50, 0\n"
    "v_mov_b32 v51, 0\n"
    "v_mov_b32 v52, 0\n"
    "v_mov_b32 v53, 0\n"
    "v_mov_b32 v54, 0\n"
    "v_mov_b32 v55, 0\n"
    "v_mov_b32 v56, 0\n"
    "v_mov_b32 v57, 0\n"
    "v_mov_b32 v58, 0\n"
    "v_mov_b32 v59, 0\n"
    "v_mov_b32 v60, 0\n"
    "v_mov_b32 v61, 0\n"
    "v_mov_b32 v62, 0\n"
    "v_mov_b32 v63, 0\n"
    "s_mul_i32 s19, s87, 122880\n"
    "s_add_u32 s19, s19, 262144\n"
    "s_mov_b32 s88, 0\n"
    "s_mov_b32 s14, 0x7f000000\n"
    "s_lshr_b32 s89, s2, 3\n"
    "s_lshl_b32 s89, s89, 12\n"
    "s_lshl_b32 s21, s36, 14\n"
    "v_add_u32 v77, s21, v255\n"
    "v_add_u32 v250, 0x10000, v77\n"
    "s_and_b32 s21, s4, 3\n"
    "s_lshl1_add_u32 s21, s21, s36\n"
    "s_lshl_b32 s21, s21, 10\n"
    "s_add_u32 s21, s21, 149504\n"
    "v_add_u32 v78, s21, v255\n"
    "v_xor_b32 v79, 0x400, v78\n"
    "s_mov_b32 s30, 0xffff\n"
    "s_mov_b32 s31, 0xffff\n"
    "s_lshl_b32 s35, s5, 9\n"
    "s_add_u32 s35, s35, 16384\n"
    "s_lshl3_add_u32 s21, s6, s4\n"
    "s_lshl3_add_u32 s34, s21, s35\n"
    "s_mul_i32 s86, s12, 22528\n"
    "s_add_u32 s86, s86, s19\n"
    "s_and_b32 s21, s4, 2\n"
    "s_add_u32 s21, s21, s36\n"
    "s_lshl2_add_u32 s21, s6, s21\n"
    "s_add_u32 s21, s21, 88\n"
    "s_lshl_b32 s21, s21, 10\n"
    "s_add_u32 s20, s21, s19\n"
    "s_mul_i32 s21, s4, 2304\n"
    "s_add_u32 s21, s21, 131072\n"
    "v_add_u32 v243, s21, v255\n"
    "v_lshl_add_u32 v244, v254, 2, s21\n"
    "s_xor_b32 s21, s4, 4\n"
    "s_mul_i32 s21, s21, 2304\n"
    "s_add_u32 s21, s21, 131072\n"
    "v_add_u32 v245, s21, v255\n"
    "v_lshl_add_u32 v246, v254, 2, s21\n"
    "v_and_b32 v247, 3, v254\n"
    "v_lshlrev_b32 v247, 2, v247\n"
    "v_add_u32 v247, 158720, v247\n"
    "v_lshlrev_b32 v127, 7, v254\n"
    "v_and_b32 v1, 31, v254\n"
    "v_lshlrev_b32 v1, 1, v1\n"
    "s_lshl_b32 s21, s39, 6\n"
    "s_add_u32 s21, s21, s17\n"
    "buffer_load_ushort v22, v1, s[64:67], s21 offen\n"
    "buffer_load_ushort v23, v1, s[64:67], s21 offen offset:64\n"
    "s_add_u32 s29, s25, 3\n"
    "s_and_b32 s29, s29, 7\n"
    "s_lshl_b32 s28, s29, 16\n"
    "s_add_u32 m0, s10, 24576\n"
    "s_add_u32 s28, s28, s16\n"
    "buffer_load_dwordx4 v253, s[44:47], s28 offen lds\n"
    "s_mul_i32 s28, s29, 176128\n"
    "s_add_u32 m0, s10, 0\n"
    "s_add_u32 s28, s28, s22\n"
    "buffer_load_dwordx4 v114, s[60:63], s28 offen lds\n"
    "s_waitcnt vmcnt(4) lgkmcnt(0)\n"
    "s_barrier\n"
    "s_lshl_b32 s23, s87, 7\n"
    "s_add_u32 s23, s23, 4096\n"
    "s_cmp_eq_u32 s12, 3\n"
    "s_cselect_b32 s23, s23, s14\n"
    "s_mov_b64 exec, 1\n"
    "v_mov_b32 v20, s8\n"
    "v_mov_b32 v21, s9\n"
    "buffer_store_dwordx2 v[20:21], off, s[76:79], s23 sc1\n"
    "s_mov_b64 exec, -1\n"
    "s_add_u32 s29, s25, 4\n"
    "s_and_b32 s29, s29, 7\n"
    "s_lshl_b32 s28, s29, 16\n"
    "s_add_u32 m0, s10, 155648\n"
    "s_add_u32 s28, s28, s16\n"
    "buffer_load_dwordx4 v253, s[44:47], s28 offen lds\n"
    "s_mul_i32 s28, s29, 176128\n"
    "s_add_u32 m0, s10, 131072\n"
    "s_add_u32 s28, s28, s22\n"
    "buffer_load_dwordx4 v114, s[60:63], s28 offen lds\n"
    "s_waitcnt vmcnt(3) lgkmcnt(0)\n"
    "s_barrier\n"
    "s_add_u32 s29, s25, 5\n"
    "s_and_b32 s29, s29, 7\n"
    "s_lshl_b32 s28, s29, 16\n"
    "s_add_u32 m0, s10, 57344\n"
    "s_add_u32 s28, s28, s16\n"
    "buffer_load_dwordx4 v253, s[44:47], s28 offen lds\n"
    "s_mul_i32 s28, s29, 176128\n"
    "s_add_u32 m0, s10, 32768\n"
    "s_add_u32 s28, s28, s22\n"
    "buffer_load_dwordx4 v114, s[60:63], s28 offen lds\n"
    "s_waitcnt vmcnt(2) lgkmcnt(0)\n"
    "s_barrier\n"
    "s_add_u32 s29, s25, 6\n"
    "s_and_b32 s29, s29, 7\n"
    "s_lshl_b32 s28, s29, 16\n"
    "s_add_u32 m0, s10, 90112\n"
    "s_add_u32 s28, s28, s16\n"
    "buffer_load_dwordx4 v253, s[44:47], s28 offen lds\n"
    "s_mul_i32 s28, s29, 176128\n"
    "s_add_u32 m0, s10, 65536\n"
    "s_add_u32 s28, s28, s22\n"
    "buffer_load_dwordx4 v114, s[60:63], s28 offen lds\n"
    "s_waitcnt vmcnt(2) lgkmcnt(0)\n"
    "s_barrier\n"
    "s_add_u32 s29, s25, 7\n"
    "s_and_b32 s29, s29, 7\n"
    "s_lshl_b32 s28, s29, 16\n"
    "s_add_u32 m0, s10, 24576\n"
    "s_add_u32 s28, s28, s16\n"
    "buffer_load_dwordx4 v253, s[44:47], s28 offen lds\n"
    "s_mul_i32 s28, s29, 176128\n"
    "s_add_u32 m0, s10, 0\n"
    "s_add_u32 s28, s28, s22\n"
    "buffer_load_dwordx4 v114, s[60:63], s28 offen lds\n"
    "s_waitcnt vmcnt(2) lgkmcnt(0)\n"
    "s_barrier\n"
    "s_waitcnt vmcnt(0) lgkmcnt(0)\n"
    "s_barrier\n"
    "s_cmp_eq_u32 s4, 7\n"
    "s_cbranch_scc0 w713_%=\n"
    "s_movk_i32 s23, 4096\n"
    "buffer_load_dwordx2 v[20:21], v127, s[76:79], s23 offen sc1\n"
    "w713_%=:\n"
    "s_waitcnt lgkmcnt(0)\n"
    "s_cmp_eq_u32 s4, 7\n"
    "s_cbranch_scc0 w715_%=\n"
    "s_movk_i32 s23, 4096\n"
    "s_waitcnt vmcnt(0)\n"
    "v_cmp_ne_u64 s[26:27], v[20:21], s[8:9]\n"
    "s_cmp_eq_u64 s[26:27], 0\n"
    "s_cbranch_scc1 ok16_%=\n"
    "s_mov_b32 s24, 0\n"
    "sp17_%=:\n"
    "buffer_load_dwordx2 v[20:21], v127, s[76:79], s23 offen sc1\n"
    "s_add_u32 s24, s24, 1\n"
    "s_waitcnt vmcnt(0)\n"
    "v_cmp_ne_u64 s[26:27], v[20:21], s[8:9]\n"
    "s_cmp_eq_u64 s[26:27], 0\n"
    "s_cbranch_scc1 ok16_%=\n"
    "s_sleep 2\n"
    "s_cmp_gt_u32 s24, 0x40000\n"
    "s_cbranch_scc0 sp17_%=\n"
    "ok16_%=:\n"
    "w715_%=:\n"
    "s_barrier\n"
    "s_lshl_b32 s21, s87, 10\n"
    "s_add_u32 s21, s21, 65536\n"
    "s_mov_b32 s88, 1\n"
    "buffer_load_dwordx4 v[4:7], v255, s[76:79], s21 offen sc1\n"
    "s_add_u32 s21, s10, 0\n"
    "s_add_u32 s23, s21, s89\n"
    "s_and_b32 s23, s23, 0x1ffff\n"
    "s_add_u32 m0, s21, 32768\n"
    "s_or_b32 s23, s23, 131072\n"
    "buffer_load_dwordx4 v255, s[76:79], s23 offen sc1 lds\n"
    "s_add_u32 s21, s10, 8192\n"
    "s_add_u32 s23, s21, s89\n"
    "s_and_b32 s23, s23, 0x1ffff\n"
    "s_add_u32 m0, s21, 32768\n"
    "s_or_b32 s23, s23, 131072\n"
    "buffer_load_dwordx4 v255, s[76:79], s23 offen sc1 lds\n"
    "s_add_u32 s21, s10, 16384\n"
    "s_add_u32 s23, s21, s89\n"
    "s_and_b32 s23, s23, 0x1ffff\n"
    "s_add_u32 m0, s21, 32768\n"
    "s_or_b32 s23, s23, 131072\n"
    "buffer_load_dwordx4 v255, s[76:79], s23 offen sc1 lds\n"
    "s_add_u32 s21, s10, 24576\n"
    "s_add_u32 s23, s21, s89\n"
    "s_and_b32 s23, s23, 0x1ffff\n"
    "s_add_u32 m0, s21, 32768\n"
    "s_or_b32 s23, s23, 131072\n"
    "buffer_load_dwordx4 v255, s[76:79], s23 offen sc1 lds\n"
    "v_mov_b32 v204, 0x3f80\n"
    "v_mov_b32 v205, 0\n"
    "v_mov_b32 v206, 0\n"
    "v_mov_b32 v207, 0\n"
    "v_mov_b32 v209, 0\n"
    "v_mov_b32 v210, 0\n"
    "v_mov_b32 v211, 0\n"
    "v_mov_b32 v213, 0\n"
    "v_mov_b32 v214, 0\n"
    "v_mov_b32 v215, 0\n"
    "s_waitcnt vmcnt(0) lgkmcnt(0)\n"
    "s_barrier\n"
    "ds_read_b128 v[192:195], v255 offset:32768\n"
    "ds_read_b128 v[80:83], v78\n"
    "ds_read_b128 v[84:87], v79\n"
    "ds_read_b128 v[88:91], v77 offset:32768\n"
    "ds_read_b128 v[92:95], v77 offset:34816\n"
    "s_mov_b64 exec, s[30:31]\n"
    "ds_read_b128 v[64:67], v77 offset:33792\n"
    "ds_read_b128 v[68:71], v77 offset:34048\n"
    "s_mov_b64 exec, -1\n"
    "s_cmp_eq_u32 s36, 0\n"
    "s_cbranch_scc1 kc18_%=\n"
    "s_setprio 1\n"
    "s_and_b32 s21, s4, 3\n"
    "s_lshl_b32 s21, s21, 12\n"
    "s_add_u32 s23, s21, s89\n"
    "s_add_u32 s23, s23, 49152\n"
    "s_and_b32 s23, s23, 0x1ffff\n"
    "s_add_u32 m0, s21, 81920\n"
    "s_or_b32 s23, s23, 131072\n"
    "buffer_load_dwordx4 v255, s[76:79], s23 offen offset:0 sc1 lds\n"
    "buffer_load_dwordx4 v255, s[76:79], s23 offen offset:1024 sc1 lds\n"
    "buffer_load_dwordx4 v255, s[76:79], s23 offen offset:2048 sc1 lds\n"
    "buffer_load_dwordx4 v255, s[76:79], s23 offen offset:3072 sc1 lds\n"
    "s_and_b32 s21, s4, 3\n"
    "s_lshl_b32 s21, s21, 12\n"
    "s_add_u32 s23, s21, s89\n"
    "s_add_u32 s23, s23, 32768\n"
    "s_and_b32 s23, s23, 0x1ffff\n"
    "s_add_u32 m0, s21, 65536\n"
    "s_or_b32 s23, s23, 131072\n"
    "buffer_load_dwordx4 v255, s[76:79], s23 offen offset:0 sc1 lds\n"
    "buffer_load_dwordx4 v255, s[76:79], s23 offen offset:1024 sc1 lds\n"
    "buffer_load_dwordx4 v255, s[76:79], s23 offen offset:2048 sc1 lds\n"
    "buffer_load_dwordx4 v255, s[76:79], s23 offen offset:3072 sc1 lds\n"
    "kc18_%=:\n"
    "s_waitcnt lgkmcnt(5)\n"
    "v_mfma_f32_32x32x16_bf16 v[96:111], v[192:195], v[80:83], 0\n"
    "s_waitcnt lgkmcnt(4)\n"
    "v_mfma_f32_32x32x16_bf16 v[112:127], v[192:195], v[84:87], 0\n"
    "s_nop 9\n"
    "v_max3_f32 v78, v96, v97, v98\n"
    "v_max3_f32 v78, v78, v99, v100\n"
    "v_max3_f32 v78, v78, v101, v102\n"
    "v_max3_f32 v78, v78, v103, v104\n"
    "v_max3_f32 v78, v78, v105, v106\n"
    "v_max3_f32 v78, v78, v107, v108\n"
    "v_max3_f32 v78, v78, v109, v110\n"
    "v_max_f32 v78, v78, v111\n"
    "v_max3_f32 v79, v112, v113, v114\n"
    "v_max3_f32 v79, v79, v115, v116\n"
    "v_max3_f32 v79, v79, v117, v118\n"
    "v_max3_f32 v79, v79, v119, v120\n"
    "v_max3_f32 v79, v79, v121, v122\n"
    "v_max3_f32 v79, v79, v123, v124\n"
    "v_max3_f32 v79, v79, v125, v126\n"
    "v_max_f32 v79, v79, v127\n"
    "v_mov_b32 v192, v78\n"
    "s_nop 1\n"
    "v_permlane32_swap_b32 v78, v192\n"
    "v_max_f32 v78, v78, v192\n"
    "v_mov_b32 v193, v79\n"
    "s_nop 1\n"
    "v_permlane32_swap_b32 v79, v193\n"
    "v_max_f32 v79, v79, v193\n"
    "v_mov_b32 v192, 0xc2340000\n"
    "v_fma_f32 v78, v78, -0.5, v192\n"
    "v_fma_f32 v79, v79, -0.5, v192\n"
    "v_cvt_pk_bf16_f32 v208, v78, v205\n"
    "v_cvt_pk_bf16_f32 v212, v79, v205\n"
    "v_mov_b32 v192, -1\n"
    "v_mov_b32 v193, -1\n"
    "v_mov_b32 v194, -1\n"
    "v_mov_b32 v195, -1\n"
    "v_mfma_f32_32x32x16_bf16 v[96:111], v[204:207], v[208:211], 0\n"
    "v_mfma_f32_32x32x16_bf16 v[112:127], v[204:207], v[212:215], 0\n"
    "s_waitcnt lgkmcnt(0)\n"
    "v_mfma_f32_32x32x16_bf16 v[0:15], v[88:91], v[80:83], v[96:111]\n"
    "s_branch at7_%=\n"
    "pf3_%=:\n"
    "s_waitcnt vmcnt(7) lgkmcnt(0)\n"
    "s_barrier\n"
    "s_add_u32 s8, s8, 1\n"
    "s_addc_u32 s9, s9, 0\n"
    "s_add_u32 s28, s25, 2\n"
    "s_and_b32 s28, s28, 7\n"
    "s_lshl_b32 s28, s28, 7\n"
    "s_add_u32 m0, s10, 81920\n"
    "s_add_u32 s28, s28, s15\n"
    "buffer_load_dwordx4 v112, s[40:43], s28 offen lds\n"
    "ds_read_b128 v[80:83], v117 offset:0\n"
    "ds_read_b64_tr_b16 v[160:161], v8 offset:0\n"
    "ds_read_b64_tr_b16 v[162:163], v8 offset:1536\n"
    "ds_read_b64_tr_b16 v[192:193], v9 offset:0\n"
    "ds_read_b64_tr_b16 v[194:195], v9 offset:1536\n"
    "ds_read_b128 v[84:87], v118 offset:0\n"
    "ds_read_b64_tr_b16 v[164:165], v8 offset:6144\n"
    "ds_read_b64_tr_b16 v[166:167], v8 offset:7680\n"
    "ds_read_b64_tr_b16 v[196:197], v9 offset:6144\n"
    "s_add_u32 s29, s25, 2\n"
    "s_and_b32 s29, s29, 7\n"
    "s_lshl_b32 s28, s29, 16\n"
    "s_add_u32 m0, s10, 57344\n"
    "s_add_u32 s28, s28, s16\n"
    "buffer_load_dwordx4 v253, s[44:47], s28 offen lds\n"
    "ds_read_b64_tr_b16 v[198:199], v9 offset:7680\n"
    "ds_read_b128 v[88:91], v119 offset:0\n"
    "ds_read_b64_tr_b16 v[168:169], v10 offset:0\n"
    "ds_read_b64_tr_b16 v[170:171], v10 offset:1536\n"
    "ds_read_b64_tr_b16 v[200:201], v11 offset:0\n"
    "ds_read_b64_tr_b16 v[202:203], v11 offset:1536\n"
    "s_mul_i32 s28, s29, 176128\n"
    "s_add_u32 m0, s22, 32768\n"
    "s_add_u32 s28, s28, s17\n"
    "buffer_load_dwordx4 v114, s[60:63], s28 offen lds\n"
    "ds_read_b128 v[92:95], v120 offset:0\n"
    "ds_read_b64_tr_b16 v[172:173], v10 offset:6144\n"
    "ds_read_b64_tr_b16 v[174:175], v10 offset:7680\n"
    "ds_read_b64_tr_b16 v[204:205], v11 offset:6144\n"
    "ds_read_b64_tr_b16 v[206:207], v11 offset:7680\n"
    "s_add_u32 m0, s22, 33792\n"
    "s_nop 0\n"
    "buffer_load_dwordx4 v115, s[60:63], s28 offen lds\n"
    "v_add_u32 v8, 131072, v121\n"
    "v_add_u32 v10, 131072, v122\n"
    "v_xor_b32 v9, 64, v8\n"
    "v_xor_b32 v11, 64, v10\n"
    "s_add_u32 m0, s22, 34816\n"
    "s_nop 0\n"
    "buffer_load_dwordx4 v116, s[60:63], s28 offen lds\n"
    "s_waitcnt vmcnt(5) lgkmcnt(0)\n"
    "s_barrier\n"
    "v_smfmac_f32_16x16x64_bf16 v[224:227], v[232:235], v[160:167], v124\n"
    "s_add_u32 s28, s25, 3\n"
    "s_and_b32 s28, s28, 7\n"
    "s_lshl_b32 s28, s28, 7\n"
    "s_add_u32 m0, s10, 90112\n"
    "s_add_u32 s28, s28, s15\n"
    "buffer_load_dwordx4 v112, s[40:43], s28 offen lds\n"
    "ds_read_b128 v[96:99], v117 offset:8192\n"
    "ds_read_b64_tr_b16 v[176:177], v8 offset:0\n"
    "ds_read_b64_tr_b16 v[178:179], v8 offset:1536\n"
    "v_mfma_f32_32x32x16_bf16 v[128:143], v[160:163], v[80:83], 0\n"
    "ds_read_b64_tr_b16 v[208:209], v9 offset:0\n"
    "ds_read_b64_tr_b16 v[210:211], v9 offset:1536\n"
    "ds_read_b128 v[100:103], v118 offset:8192\n"
    "v_smfmac_f32_16x16x64_bf16 v[228:231], v[232:235], v[192:199], v124\n"
    "ds_read_b64_tr_b16 v[180:181], v8 offset:6144\n"
    "ds_read_b64_tr_b16 v[182:183], v8 offset:7680\n"
    "ds_read_b64_tr_b16 v[212:213], v9 offset:6144\n"
    "v_mfma_f32_32x32x16_bf16 v[144:159], v[192:195], v[80:83], 0\n"
    "s_add_u32 s29, s25, 3\n"
    "s_and_b32 s29, s29, 7\n"
    "s_lshl_b32 s28, s29, 16\n"
    "s_add_u32 m0, s10, 24576\n"
    "s_add_u32 s28, s28, s16\n"
    "buffer_load_dwordx4 v253, s[44:47], s28 offen lds\n"
    "v_mfma_f32_32x32x16_bf16 v[128:143], v[164:167], v[84:87], v[128:143]\n"
    "ds_read_b64_tr_b16 v[214:215], v9 offset:7680\n"
    "ds_read_b128 v[104:107], v119 offset:8192\n"
    "ds_read_b64_tr_b16 v[184:185], v10 offset:0\n"
    "v_mfma_f32_32x32x16_bf16 v[144:159], v[196:199], v[84:87], v[144:159]\n"
    "ds_read_b64_tr_b16 v[186:187], v10 offset:1536\n"
    "ds_read_b64_tr_b16 v[216:217], v11 offset:0\n"
    "ds_read_b64_tr_b16 v[218:219], v11 offset:1536\n"
    "s_mul_i32 s28, s29, 176128\n"
    "s_add_u32 m0, s22, 0\n"
    "s_add_u32 s28, s28, s17\n"
    "buffer_load_dwordx4 v114, s[60:63], s28 offen lds\n"
    "v_mfma_f32_32x32x16_bf16 v[128:143], v[168:171], v[88:91], v[128:143]\n"
    "ds_read_b128 v[108:111], v120 offset:8192\n"
    "ds_read_b64_tr_b16 v[188:189], v10 offset:6144\n"
    "ds_read_b64_tr_b16 v[190:191], v10 offset:7680\n"
    "v_mfma_f32_32x32x16_bf16 v[144:159], v[200:203], v[88:91], v[144:159]\n"
    "ds_read_b64_tr_b16 v[220:221], v11 offset:6144\n"
    "ds_read_b64_tr_b16 v[222:223], v11 offset:7680\n"
    "s_add_u32 m0, s22, 1024\n"
    "s_nop 0\n"
    "buffer_load_dwordx4 v115, s[60:63], s28 offen lds\n"
    "v_mfma_f32_32x32x16_bf16 v[128:143], v[172:175], v[92:95], v[128:143]\n"
    "v_add_u32 v8, 32768, v121\n"
    "v_add_u32 v10, 32768, v122\n"
    "v_xor_b32 v9, 64, v8\n"
    "v_xor_b32 v11, 64, v10\n"
    "v_mfma_f32_32x32x16_bf16 v[144:159], v[204:207], v[92:95], v[144:159]\n"
    "s_add_u32 m0, s22, 2048\n"
    "s_nop 0\n"
    "buffer_load_dwordx4 v116, s[60:63], s28 offen lds\n"
    "s_waitcnt vmcnt(5) lgkmcnt(0)\n"
    "s_barrier\n"
    "v_smfmac_f32_16x16x64_bf16 v[224:227], v[232:235], v[176:183], v124\n"
    "s_add_u32 s28, s25, 4\n"
    "s_and_b32 s28, s28, 7\n"
    "s_lshl_b32 s28, s28, 7\n"
    "s_add_u32 m0, s10, 98304\n"
    "s_add_u32 s28, s28, s15\n"
    "buffer_load_dwordx4 v112, s[40:43], s28 offen lds\n"
    "ds_read_b128 v[80:83], v117 offset:16384\n"
    "ds_read_b64_tr_b16 v[160:161], v8 offset:0\n"
    "ds_read_b64_tr_b16 v[162:163], v8 offset:1536\n"
    "v_mfma_f32_32x32x16_bf16 v[128:143], v[176:179], v[96:99], v[128:143]\n"
    "ds_read_b64_tr_b16 v[192:193], v9 offset:0\n"
    "ds_read_b64_tr_b16 v[194:195], v9 offset:1536\n"
    "ds_read_b128 v[84:87], v118 offset:16384\n"
    "v_smfmac_f32_16x16x64_bf16 v[228:231], v[232:235], v[208:215], v124\n"
    "ds_read_b64_tr_b16 v[164:165], v8 offset:6144\n"
    "ds_read_b64_tr_b16 v[166:167], v8 offset:7680\n"
    "ds_read_b64_tr_b16 v[196:197], v9 offset:6144\n"
    "v_mfma_f32_32x32x16_bf16 v[144:159], v[208:211], v[96:99], v[144:159]\n"
    "s_add_u32 s29, s25, 4\n"
    "s_and_b32 s29, s29, 7\n"
    "s_lshl_b32 s28, s29, 16\n"
    "s_add_u32 m0, s10, 155648\n"
    "s_add_u32 s28, s28, s16\n"
    "buffer_load_dwordx4 v253, s[44:47], s28 offen lds\n"
    "v_mfma_f32_32x32x16_bf16 v[128:143], v[180:183], v[100:103], v[128:143]\n"
    "ds_read_b64_tr_b16 v[198:199], v9 offset:7680\n"
    "ds_read_b128 v[88:91], v119 offset:16384\n"
    "ds_read_b64_tr_b16 v[168:169], v10 offset:0\n"
    "v_mfma_f32_32x32x16_bf16 v[144:159], v[212:215], v[100:103], v[144:159]\n"
    "ds_read_b64_tr_b16 v[170:171], v10 offset:1536\n"
    "ds_read_b64_tr_b16 v[200:201], v11 offset:0\n"
    "ds_read_b64_tr_b16 v[202:203], v11 offset:1536\n"
    "s_mul_i32 s28, s29, 176128\n"
    "s_add_u32 m0, s22, 131072\n"
    "s_add_u32 s28, s28, s17\n"
    "buffer_load_dwordx4 v114, s[60:63], s28 offen lds\n"
    "v_mfma_f32_32x32x16_bf16 v[128:143], v[184:187], v[104:107], v[128:143]\n"
    "ds_read_b128 v[92:95], v120 offset:16384\n"
    "ds_read_b64_tr_b16 v[172:173], v10 offset:6144\n"
    "ds_read_b64_tr_b16 v[174:175], v10 offset:7680\n"
    "v_mfma_f32_32x32x16_bf16 v[144:159], v[216:219], v[104:107], v[144:159]\n"
    "ds_read_b64_tr_b16 v[204:205], v11 offset:6144\n"
    "ds_read_b64_tr_b16 v[206:207], v11 offset:7680\n"
    "s_add_u32 m0, s22, 132096\n"
    "s_nop 0\n"
    "buffer_load_dwordx4 v115, s[60:63], s28 offen lds\n"
    "v_mfma_f32_32x32x16_bf16 v[128:143], v[188:191], v[108:111], v[128:143]\n"
    "v_add_u32 v8, 0, v121\n"
    "v_add_u32 v10, 0, v122\n"
    "v_xor_b32 v9, 64, v8\n"
    "v_xor_b32 v11, 64, v10\n"
    "v_mfma_f32_32x32x16_bf16 v[144:159], v[220:223], v[108:111], v[144:159]\n"
    "s_add_u32 m0, s22, 133120\n"
    "s_nop 0\n"
    "buffer_load_dwordx4 v116, s[60:63], s28 offen lds\n"
    "s_waitcnt vmcnt(5) lgkmcnt(0)\n"
    "s_barrier\n"
    "v_smfmac_f32_16x16x64_bf16 v[224:227], v[232:235], v[160:167], v124\n"
    "s_add_u32 s28, s25, 5\n"
    "s_and_b32 s28, s28, 7\n"
    "s_lshl_b32 s28, s28, 7\n"
    "s_add_u32 m0, s10, 106496\n"
    "s_add_u32 s28, s28, s15\n"
    "buffer_load_dwordx4 v112, s[40:43], s28 offen lds\n"
    "ds_read_b128 v[96:99], v117 offset:24576\n"
    "ds_read_b64_tr_b16 v[176:177], v8 offset:0\n"
    "ds_read_b64_tr_b16 v[178:179], v8 offset:1536\n"
    "v_mfma_f32_32x32x16_bf16 v[128:143], v[160:163], v[80:83], v[128:143]\n"
    "ds_read_b64_tr_b16 v[208:209], v9 offset:0\n"
    "ds_read_b64_tr_b16 v[210:211], v9 offset:1536\n"
    "ds_read_b128 v[100:103], v118 offset:24576\n"
    "v_smfmac_f32_16x16x64_bf16 v[228:231], v[232:235], v[192:199], v124\n"
    "ds_read_b64_tr_b16 v[180:181], v8 offset:6144\n"
    "ds_read_b64_tr_b16 v[182:183], v8 offset:7680\n"
    "ds_read_b64_tr_b16 v[212:213], v9 offset:6144\n"
    "v_mfma_f32_32x32x16_bf16 v[144:159], v[192:195], v[80:83], v[144:159]\n"
    "s_add_u32 s29, s25, 5\n"
    "s_and_b32 s29, s29, 7\n"
    "s_lshl_b32 s28, s29, 16\n"
    "s_add_u32 m0, s10, 57344\n"
    "s_add_u32 s28, s28, s16\n"
    "buffer_load_dwordx4 v253, s[44:47], s28 offen lds\n"
    "v_mfma_f32_32x32x16_bf16 v[128:143], v[164:167], v[84:87], v[128:143]\n"
    "ds_read_b64_tr_b16 v[214:215], v9 offset:7680\n"
    "ds_read_b128 v[104:107], v119 offset:24576\n"
    "ds_read_b64_tr_b16 v[184:185], v10 offset:0\n"
    "v_mfma_f32_32x32x16_bf16 v[144:159], v[196:199], v[84:87], v[144:159]\n"
    "ds_read_b64_tr_b16 v[186:187], v10 offset:1536\n"
    "ds_read_b64_tr_b16 v[216:217], v11 offset:0\n"
    "ds_read_b64_tr_b16 v[218:219], v11 offset:1536\n"
    "s_mul_i32 s28, s29, 176128\n"
    "s_add_u32 m0, s22, 32768\n"
    "s_add_u32 s28, s28, s17\n"
    "buffer_load_dwordx4 v114, s[60:63], s28 offen lds\n"
    "v_mfma_f32_32x32x16_bf16 v[128:143], v[168:171], v[88:91], v[128:143]\n"
    "ds_read_b128 v[108:111], v120 offset:24576\n"
    "ds_read_b64_tr_b16 v[188:189], v10 offset:6144\n"
    "ds_read_b64_tr_b16 v[190:191], v10 offset:7680\n"
    "v_mfma_f32_32x32x16_bf16 v[144:159], v[200:203], v[88:91], v[144:159]\n"
    "ds_read_b64_tr_b16 v[220:221], v11 offset:6144\n"
    "ds_read_b64_tr_b16 v[222:223], v11 offset:7680\n"
    "s_add_u32 m0, s22, 33792\n"
    "s_nop 0\n"
    "buffer_load_dwordx4 v115, s[60:63], s28 offen lds\n"
    "v_mfma_f32_32x32x16_bf16 v[128:143], v[172:175], v[92:95], v[128:143]\n"
    "v_add_u32 v8, 131072, v121\n"
    "v_add_u32 v10, 131072, v122\n"
    "v_xor_b32 v9, 64, v8\n"
    "v_xor_b32 v11, 64, v10\n"
    "v_mfma_f32_32x32x16_bf16 v[144:159], v[204:207], v[92:95], v[144:159]\n"
    "s_add_u32 m0, s22, 34816\n"
    "s_nop 0\n"
    "buffer_load_dwordx4 v116, s[60:63], s28 offen lds\n"
    "s_waitcnt vmcnt(5) lgkmcnt(0)\n"
    "s_barrier\n"
    "v_smfmac_f32_16x16x64_bf16 v[224:227], v[232:235], v[176:183], v124\n"
    "s_add_u32 s28, s25, 6\n"
    "s_and_b32 s28, s28, 7\n"
    "s_lshl_b32 s28, s28, 7\n"
    "s_add_u32 m0, s10, 114688\n"
    "s_add_u32 s28, s28, s15\n"
    "buffer_load_dwordx4 v112, s[40:43], s28 offen lds\n"
    "ds_read_b128 v[80:83], v117 offset:32768\n"
    "ds_read_b64_tr_b16 v[160:161], v8 offset:0\n"
    "ds_read_b64_tr_b16 v[162:163], v8 offset:1536\n"
    "v_mfma_f32_32x32x16_bf16 v[128:143], v[176:179], v[96:99], v[128:143]\n"
    "ds_read_b64_tr_b16 v[192:193], v9 offset:0\n"
    "ds_read_b64_tr_b16 v[194:195], v9 offset:1536\n"
    "ds_read_b128 v[84:87], v118 offset:32768\n"
    "v_smfmac_f32_16x16x64_bf16 v[228:231], v[232:235], v[208:215], v124\n"
    "ds_read_b64_tr_b16 v[164:165], v8 offset:6144\n"
    "ds_read_b64_tr_b16 v[166:167], v8 offset:7680\n"
    "ds_read_b64_tr_b16 v[196:197], v9 offset:6144\n"
    "v_mfma_f32_32x32x16_bf16 v[144:159], v[208:211], v[96:99], v[144:159]\n"
    "s_add_u32 s29, s25, 6\n"
    "s_and_b32 s29, s29, 7\n"
    "s_lshl_b32 s28, s29, 16\n"
    "s_add_u32 m0, s10, 90112\n"
    "s_add_u32 s28, s28, s16\n"
    "buffer_load_dwordx4 v253, s[44:47], s28 offen lds\n"
    "v_mfma_f32_32x32x16_bf16 v[128:143], v[180:183], v[100:103], v[128:143]\n"
    "ds_read_b64_tr_b16 v[198:199], v9 offset:7680\n"
    "ds_read_b128 v[88:91], v119 offset:32768\n"
    "ds_read_b64_tr_b16 v[168:169], v10 offset:0\n"
    "v_mfma_f32_32x32x16_bf16 v[144:159], v[212:215], v[100:103], v[144:159]\n"
    "ds_read_b64_tr_b16 v[170:171], v10 offset:1536\n"
    "ds_read_b64_tr_b16 v[200:201], v11 offset:0\n"
    "ds_read_b64_tr_b16 v[202:203], v11 offset:1536\n"
    "s_mul_i32 s28, s29, 176128\n"
    "s_add_u32 m0, s22, 65536\n"
    "s_add_u32 s28, s28, s17\n"
    "buffer_load_dwordx4 v114, s[60:63], s28 offen lds\n"
    "v_mfma_f32_32x32x16_bf16 v[128:143], v[184:187], v[104:107], v[128:143]\n"
    "ds_read_b128 v[92:95], v120 offset:32768\n"
    "ds_read_b64_tr_b16 v[172:173], v10 offset:6144\n"
    "ds_read_b64_tr_b16 v[174:175], v10 offset:7680\n"
    "v_mfma_f32_32x32x16_bf16 v[144:159], v[216:219], v[104:107], v[144:159]\n"
    "ds_read_b64_tr_b16 v[204:205], v11 offset:6144\n"
    "ds_read_b64_tr_b16 v[206:207], v11 offset:7680\n"
    "s_add_u32 m0, s22, 66560\n"
    "s_nop 0\n"
    "buffer_load_dwordx4 v115, s[60:63], s28 offen lds\n"
    "v_mfma_f32_32x32x16_bf16 v[128:143], v[188:191], v[108:111], v[128:143]\n"
    "v_add_u32 v8, 32768, v121\n"
    "v_add_u32 v10, 32768, v122\n"
    "v_xor_b32 v9, 64, v8\n"
    "v_xor_b32 v11, 64, v10\n"
    "v_mfma_f32_32x32x16_bf16 v[144:159], v[220:223], v[108:111], v[144:159]\n"
    "s_add_u32 m0, s22, 67584\n"
    "s_nop 0\n"
    "buffer_load_dwordx4 v116, s[60:63], s28 offen lds\n"
    "s_waitcnt vmcnt(5) lgkmcnt(0)\n"
    "s_barrier\n"
    "v_smfmac_f32_16x16x64_bf16 v[224:227], v[232:235], v[160:167], v124\n"
    "s_add_u32 s28, s25, 7\n"
    "s_and_b32 s28, s28, 7\n"
    "s_lshl_b32 s28, s28, 7\n"
    "s_add_u32 m0, s10, 122880\n"
    "s_add_u32 s28, s28, s15\n"
    "buffer_load_dwordx4 v112, s[40:43], s28 offen lds\n"
    "ds_read_b128 v[96:99], v117 offset:40960\n"
    "ds_read_b64_tr_b16 v[176:177], v8 offset:0\n"
    "ds_read_b64_tr_b16 v[178:179], v8 offset:1536\n"
    "v_mfma_f32_32x32x16_bf16 v[128:143], v[160:163], v[80:83], v[128:143]\n"
    "ds_read_b64_tr_b16 v[208:209], v9 offset:0\n"
    "ds_read_b64_tr_b16 v[210:211], v9 offset:1536\n"
    "ds_read_b128 v[100:103], v118 offset:40960\n"
    "v_smfmac_f32_16x16x64_bf16 v[228:231], v[232:235], v[192:199], v124\n"
    "ds_read_b64_tr_b16 v[180:181], v8 offset:6144\n"
    "ds_read_b64_tr_b16 v[182:183], v8 offset:7680\n"
    "ds_read_b64_tr_b16 v[212:213], v9 offset:6144\n"
    "v_mfma_f32_32x32x16_bf16 v[144:159], v[192:195], v[80:83], v[144:159]\n"
    "s_add_u32 s29, s25, 7\n"
    "s_and_b32 s29, s29, 7\n"
    "s_lshl_b32 s28, s29, 16\n"
    "s_add_u32 m0, s10, 24576\n"
    "s_add_u32 s28, s28, s16\n"
    "buffer_load_dwordx4 v253, s[44:47], s28 offen lds\n"
    "v_mfma_f32_32x32x16_bf16 v[128:143], v[164:167], v[84:87], v[128:143]\n"
    "ds_read_b64_tr_b16 v[214:215], v9 offset:7680\n"
    "ds_read_b128 v[104:107], v119 offset:40960\n"
    "ds_read_b64_tr_b16 v[184:185], v10 offset:0\n"
    "v_mfma_f32_32x32x16_bf16 v[144:159], v[196:199], v[84:87], v[144:159]\n"
    "ds_read_b64_tr_b16 v[186:187], v10 offset:1536\n"
    "ds_read_b64_tr_b16 v[216:217], v11 offset:0\n"
    "ds_read_b64_tr_b16 v[218:219], v11 offset:1536\n"
    "s_mul_i32 s28, s29, 176128\n"
    "s_add_u32 m0, s22, 0\n"
    "s_add_u32 s28, s28, s17\n"
    "buffer_load_dwordx4 v114, s[60:63], s28 offen lds\n"
    "v_mfma_f32_32x32x16_bf16 v[128:143], v[168:171], v[88:91], v[128:143]\n"
    "ds_read_b128 v[108:111], v120 offset:40960\n"
    "ds_read_b64_tr_b16 v[188:189], v10 offset:6144\n"
    "ds_read_b64_tr_b16 v[190:191], v10 offset:7680\n"
    "v_mfma_f32_32x32x16_bf16 v[144:159], v[200:203], v[88:91], v[144:159]\n"
    "ds_read_b64_tr_b16 v[220:221], v11 offset:6144\n"
    "ds_read_b64_tr_b16 v[222:223], v11 offset:7680\n"
    "s_add_u32 m0, s22, 1024\n"
    "s_nop 0\n"
    "buffer_load_dwordx4 v115, s[60:63], s28 offen lds\n"
    "v_mfma_f32_32x32x16_bf16 v[128:143], v[172:175], v[92:95], v[128:143]\n"
    "v_add_u32 v8, 65536, v121\n"
    "v_add_u32 v10, 65536, v122\n"
    "v_xor_b32 v9, 64, v8\n"
    "v_xor_b32 v11, 64, v10\n"
    "v_mfma_f32_32x32x16_bf16 v[144:159], v[204:207], v[92:95], v[144:159]\n"
    "s_add_u32 m0, s22, 2048\n"
    "s_nop 0\n"
    "buffer_load_dwordx4 v116, s[60:63], s28 offen lds\n"
    "s_waitcnt vmcnt(5) lgkmcnt(0)\n"
    "s_barrier\n"
    "v_smfmac_f32_16x16x64_bf16 v[224:227], v[232:235], v[176:183], v124\n"
    "ds_read_b128 v[80:83], v117 offset:49152\n"
    "ds_read_b64_tr_b16 v[160:161], v8 offset:0\n"
    "ds_read_b64_tr_b16 v[162:163], v8 offset:1536\n"
    "v_mfma_f32_32x32x16_bf16 v[128:143], v[176:179], v[96:99], v[128:143]\n"
    "ds_read_b64_tr_b16 v[192:193], v9 offset:0\n"
    "ds_read_b64_tr_b16 v[194:195], v9 offset:1536\n"
    "ds_read_b128 v[84:87], v118 offset:49152\n"
    "v_smfmac_f32_16x16x64_bf16 v[228:231], v[232:235], v[208:215], v124\n"
    "ds_read_b64_tr_b16 v[164:165], v8 offset:6144\n"
    "ds_read_b64_tr_b16 v[166:167], v8 offset:7680\n"
    "ds_read_b64_tr_b16 v[196:197], v9 offset:6144\n"
    "v_mfma_f32_32x32x16_bf16 v[144:159], v[208:211], v[96:99], v[144:159]\n"
    "v_mfma_f32_32x32x16_bf16 v[128:143], v[180:183], v[100:103], v[128:143]\n"
    "ds_read_b64_tr_b16 v[198:199], v9 offset:7680\n"
    "ds_read_b128 v[88:91], v119 offset:49152\n"
    "ds_read_b64_tr_b16 v[168:169], v10 offset:0\n"
    "v_mfma_f32_32x32x16_bf16 v[144:159], v[212:215], v[100:103], v[144:159]\n"
    "ds_read_b64_tr_b16 v[170:171], v10 offset:1536\n"
    "ds_read_b64_tr_b16 v[200:201], v11 offset:0\n"
    "ds_read_b64_tr_b16 v[202:203], v11 offset:1536\n"
    "v_mfma_f32_32x32x16_bf16 v[128:143], v[184:187], v[104:107], v[128:143]\n"
    "ds_read_b128 v[92:95], v120 offset:49152\n"
    "ds_read_b64_tr_b16 v[172:173], v10 offset:6144\n"
    "ds_read_b64_tr_b16 v[174:175], v10 offset:7680\n"
    "v_mfma_f32_32x32x16_bf16 v[144:159], v[216:219], v[104:107], v[144:159]\n"
    "ds_read_b64_tr_b16 v[204:205], v11 offset:6144\n"
    "ds_read_b64_tr_b16 v[206:207], v11 offset:7680\n"
    "v_mfma_f32_32x32x16_bf16 v[128:143], v[188:191], v[108:111], v[128:143]\n"
    "v_add_u32 v8, 0, v121\n"
    "v_add_u32 v10, 0, v122\n"
    "v_xor_b32 v9, 64, v8\n"
    "v_xor_b32 v11, 64, v10\n"
    "v_mfma_f32_32x32x16_bf16 v[144:159], v[220:223], v[108:111], v[144:159]\n"
    "s_waitcnt vmcnt(0) lgkmcnt(0)\n"
    "s_barrier\n"
    "v_smfmac_f32_16x16x64_bf16 v[224:227], v[232:235], v[160:167], v124\n"
    "s_cmp_eq_u32 s4, 7\n"
    "s_cbranch_scc0 w719_%=\n"
    "s_movk_i32 s23, 4096\n"
    "buffer_load_dwordx2 v[20:21], v127, s[76:79], s23 offen sc1\n"
    "w719_%=:\n"
    "ds_read_b128 v[96:99], v117 offset:57344\n"
    "ds_read_b64_tr_b16 v[176:177], v8 offset:0\n"
    "ds_read_b64_tr_b16 v[178:179], v8 offset:1536\n"
    "v_mfma_f32_32x32x16_bf16 v[128:143], v[160:163], v[80:83], v[128:143]\n"
    "ds_read_b64_tr_b16 v[208:209], v9 offset:0\n"
    "ds_read_b64_tr_b16 v[210:211], v9 offset:1536\n"
    "ds_read_b128 v[100:103], v118 offset:57344\n"
    "v_smfmac_f32_16x16x64_bf16 v[228:231], v[232:235], v[192:199], v124\n"
    "ds_read_b64_tr_b16 v[180:181], v8 offset:6144\n"
    "ds_read_b64_tr_b16 v[182:183], v8 offset:7680\n"
    "ds_read_b64_tr_b16 v[212:213], v9 offset:6144\n"
    "v_mfma_f32_32x32x16_bf16 v[144:159], v[192:195], v[80:83], v[144:159]\n"
    "v_mfma_f32_32x32x16_bf16 v[128:143], v[164:167], v[84:87], v[128:143]\n"
    "ds_read_b64_tr_b16 v[214:215], v9 offset:7680\n"
    "ds_read_b128 v[104:107], v119 offset:57344\n"
    "ds_read_b64_tr_b16 v[184:185], v10 offset:0\n"
    "v_mfma_f32_32x32x16_bf16 v[144:159], v[196:199], v[84:87], v[144:159]\n"
    "ds_read_b64_tr_b16 v[186:187], v10 offset:1536\n"
    "ds_read_b64_tr_b16 v[216:217], v11 offset:0\n"
    "ds_read_b64_tr_b16 v[218:219], v11 offset:1536\n"
    "v_mfma_f32_32x32x16_bf16 v[128:143], v[168:171], v[88:91], v[128:143]\n"
    "ds_read_b128 v[108:111], v120 offset:57344\n"
    "ds_read_b64_tr_b16 v[188:189], v10 offset:6144\n"
    "ds_read_b64_tr_b16 v[190:191], v10 offset:7680\n"
    "v_mfma_f32_32x32x16_bf16 v[144:159], v[200:203], v[88:91], v[144:159]\n"
    "ds_read_b64_tr_b16 v[220:221], v11 offset:6144\n"
    "ds_read_b64_tr_b16 v[222:223], v11 offset:7680\n"
    "v_mfma_f32_32x32x16_bf16 v[128:143], v[172:175], v[92:95], v[128:143]\n"
    "v_add_f32 v0, v224, v225\n"
    "v_add_f32 v1, v226, v227\n"
    "v_add_f32 v2, v228, v229\n"
    "v_add_f32 v3, v230, v231\n"
    "v_permlane16_swap_b32 v0, v1\n"
    "s_nop 0\n"
    "v_permlane16_swap_b32 v2, v3\n"
    "s_mov_b32 exec_hi, 0\n"
    "ds_write2_b32 v123, v0, v2 offset1:32\n"
    "s_mov_b32 exec_hi, -1\n"
    "v_xor_b32 v123, 0x100, v123\n"
    "v_mfma_f32_32x32x16_bf16 v[144:159], v[204:207], v[92:95], v[144:159]\n"
    "pp9_%=:\n"
    "s_waitcnt lgkmcnt(0)\n"
    "s_cmp_eq_u32 s4, 7\n"
    "s_cbranch_scc0 w721_%=\n"
    "s_movk_i32 s23, 4096\n"
    "s_waitcnt vmcnt(0)\n"
    "v_cmp_ne_u64 s[26:27], v[20:21], s[8:9]\n"
    "s_cmp_eq_u64 s[26:27], 0\n"
    "s_cbranch_scc1 ok22_%=\n"
    "s_mov_b32 s24, 0\n"
    "sp23_%=:\n"
    "buffer_load_dwordx2 v[20:21], v127, s[76:79], s23 offen sc1\n"
    "s_add_u32 s24, s24, 1\n"
    "s_waitcnt vmcnt(0)\n"
    "v_cmp_ne_u64 s[26:27], v[20:21], s[8:9]\n"
    "s_cmp_eq_u64 s[26:27], 0\n"
    "s_cbranch_scc1 ok22_%=\n"
    "s_sleep 2\n"
    "s_cmp_gt_u32 s24, 0x40000\n"
    "s_cbranch_scc0 sp23_%=\n"
    "ok22_%=:\n"
    "w721_%=:\n"
    "s_barrier\n"
    "s_lshl_b32 s21, s87, 10\n"
    "s_add_u32 s21, s21, 65536\n"
    "s_mov_b32 s88, 1\n"
    "buffer_load_dwordx4 v[4:7], v255, s[76:79], s21 offen sc1\n"
    "s_add_u32 s21, s10, 0\n"
    "s_add_u32 s23, s21, s89\n"
    "s_and_b32 s23, s23, 0x1ffff\n"
    "s_add_u32 m0, s21, 32768\n"
    "s_or_b32 s23, s23, 131072\n"
    "buffer_load_dwordx4 v255, s[76:79], s23 offen sc1 lds\n"
    "s_add_u32 s21, s10, 8192\n"
    "s_add_u32 s23, s21, s89\n"
    "s_and_b32 s23, s23, 0x1ffff\n"
    "s_add_u32 m0, s21, 32768\n"
    "s_or_b32 s23, s23, 131072\n"
    "buffer_load_dwordx4 v255, s[76:79], s23 offen sc1 lds\n"
    "s_add_u32 s21, s10, 16384\n"
    "s_add_u32 s23, s21, s89\n"
    "s_and_b32 s23, s23, 0x1ffff\n"
    "s_add_u32 m0, s21, 32768\n"
    "s_or_b32 s23, s23, 131072\n"
    "buffer_load_dwordx4 v255, s[76:79], s23 offen sc1 lds\n"
    "s_add_u32 s21, s10, 24576\n"
    "s_add_u32 s23, s21, s89\n"
    "s_and_b32 s23, s23, 0x1ffff\n"
    "s_add_u32 m0, s21, 32768\n"
    "s_or_b32 s23, s23, 131072\n"
    "buffer_load_dwordx4 v255, s[76:79], s23 offen sc1 lds\n"
    "ds_read2_b32 v[2:3], v123 offset1:32\n"
    "v_smfmac_f32_16x16x64_bf16 v[224:227], v[232:235], v[176:183], v124\n"
    "v_smfmac_f32_16x16x64_bf16 v[228:231], v[232:235], v[208:215], v124\n"
    "v_smfmac_f32_16x16x64_bf16 v[224:227], v[232:235], v[184:191], v124\n"
    "v_smfmac_f32_16x16x64_bf16 v[228:231], v[232:235], v[216:223], v124\n"
    "v_mfma_f32_32x32x16_bf16 v[128:143], v[176:179], v[96:99], v[128:143]\n"
    "v_mfma_f32_32x32x16_bf16 v[144:159], v[208:211], v[96:99], v[144:159]\n"
    "s_waitcnt lgkmcnt(0)\n"
    "v_lshlrev_b32 v22, 16, v22\n"
    "v_lshlrev_b32 v23, 16, v23\n"
    "s_nop 1\n"
    "v_add_f32 v224, v224, v225\n"
    "v_mfma_f32_32x32x16_bf16 v[128:143], v[180:183], v[100:103], v[128:143]\n"
    "v_add_f32 v225, v226, v227\n"
    "s_nop 1\n"
    "v_permlane16_swap_b32 v224, v225\n"
    "v_add_f32 v224, v224, v2\n"
    "v_mov_b32 v15, 0\n"
    "v_mfma_f32_32x32x16_bf16 v[144:159], v[212:215], v[100:103], v[144:159]\n"
    "v_cvt_pk_bf16_f32 v12, v224, v224\n"
    "v_lshlrev_b32 v225, 16, v12\n"
    "v_sub_f32 v224, v224, v225\n"
    "v_cvt_pk_bf16_f32 v14, v22, v15\n"
    "v_mfma_f32_32x32x16_bf16 v[128:143], v[184:187], v[104:107], v[128:143]\n"
    "v_cvt_pk_bf16_f32 v13, v224, v22\n"
    "v_add_f32 v228, v228, v229\n"
    "v_add_f32 v229, v230, v231\n"
    "s_nop 1\n"
    "v_permlane16_swap_b32 v228, v229\n"
    "v_mfma_f32_32x32x16_bf16 v[144:159], v[216:219], v[104:107], v[144:159]\n"
    "v_add_f32 v228, v228, v3\n"
    "v_mov_b32 v19, 0\n"
    "v_cvt_pk_bf16_f32 v16, v228, v228\n"
    "v_lshlrev_b32 v229, 16, v16\n"
    "v_mfma_f32_32x32x16_bf16 v[128:143], v[188:191], v[108:111], v[128:143]\n"
    "v_sub_f32 v228, v228, v229\n"
    "v_cvt_pk_bf16_f32 v18, v23, v19\n"
    "v_cvt_pk_bf16_f32 v17, v228, v23\n"
    "v_mfma_f32_32x32x16_bf16 v[144:159], v[220:223], v[108:111], v[144:159]\n"
    "s_waitcnt vmcnt(4)\n"
    "v_mov_b32 v160, v7\n"
    "v_mov_b32 v7, 0\n"
    "s_nop 1\n"
    "v_mfma_f32_32x32x16_bf16 v[128:143], v[12:15], v[4:7], v[128:143]\n"
    "v_mfma_f32_32x32x16_bf16 v[144:159], v[16:19], v[4:7], v[144:159]\n"
    "v_mov_b32 v204, 0x3f80\n"
    "v_mov_b32 v205, 0\n"
    "v_mov_b32 v206, 0\n"
    "v_mov_b32 v207, 0\n"
    "v_mov_b32 v209, 0\n"
    "v_mov_b32 v210, 0\n"
    "v_mov_b32 v211, 0\n"
    "v_mov_b32 v213, 0\n"
    "v_mov_b32 v214, 0\n"
    "v_mov_b32 v215, 0\n"
    "s_nop 0\n"
    "v_max_f32 v128, 0, v128\n"
    "v_max_f32 v129, 0, v129\n"
    "v_max_f32 v130, 0, v130\n"
    "v_max_f32 v131, 0, v131\n"
    "v_max_f32 v132, 0, v132\n"
    "v_max_f32 v133, 0, v133\n"
    "v_max_f32 v134, 0, v134\n"
    "v_max_f32 v135, 0, v135\n"
    "v_max_f32 v136, 0, v136\n"
    "v_max_f32 v137, 0, v137\n"
    "v_max_f32 v138, 0, v138\n"
    "v_max_f32 v139, 0, v139\n"
    "v_max_f32 v140, 0, v140\n"
    "v_max_f32 v141, 0, v141\n"
    "v_max_f32 v142, 0, v142\n"
    "v_max_f32 v143, 0, v143\n"
    "v_pk_mul_f32 v[128:129], v[128:129], v[160:161] op_sel:[0,0] op_sel_hi:[1,0]\n"
    "v_pk_mul_f32 v[130:131], v[130:131], v[160:161] op_sel:[0,0] op_sel_hi:[1,0]\n"
    "v_pk_mul_f32 v[132:133], v[132:133], v[160:161] op_sel:[0,0] op_sel_hi:[1,0]\n"
    "v_pk_mul_f32 v[134:135], v[134:135], v[160:161] op_sel:[0,0] op_sel_hi:[1,0]\n"
    "v_pk_mul_f32 v[136:137], v[136:137], v[160:161] op_sel:[0,0] op_sel_hi:[1,0]\n"
    "v_pk_mul_f32 v[138:139], v[138:139], v[160:161] op_sel:[0,0] op_sel_hi:[1,0]\n"
    "v_pk_mul_f32 v[140:141], v[140:141], v[160:161] op_sel:[0,0] op_sel_hi:[1,0]\n"
    "v_pk_mul_f32 v[142:143], v[142:143], v[160:161] op_sel:[0,0] op_sel_hi:[1,0]\n"
    "v_pk_mul_f32 v[128:129], v[128:129], v[128:129] op_sel:[0,0] op_sel_hi:[1,1]\n"
    "v_pk_mul_f32 v[130:131], v[130:131], v[130:131] op_sel:[0,0] op_sel_hi:[1,1]\n"
    "v_pk_mul_f32 v[132:133], v[132:133], v[132:133] op_sel:[0,0] op_sel_hi:[1,1]\n"
    "v_pk_mul_f32 v[134:135], v[134:135], v[134:135] op_sel:[0,0] op_sel_hi:[1,1]\n"
    "v_pk_mul_f32 v[136:137], v[136:137], v[136:137] op_sel:[0,0] op_sel_hi:[1,1]\n"
    "v_pk_mul_f32 v[138:139], v[138:139], v[138:139] op_sel:[0,0] op_sel_hi:[1,1]\n"
    "v_pk_mul_f32 v[140:141], v[140:141], v[140:141] op_sel:[0,0] op_sel_hi:[1,1]\n"
    "v_pk_mul_f32 v[142:143], v[142:143], v[142:143] op_sel:[0,0] op_sel_hi:[1,1]\n"
    "v_cvt_pk_bf16_f32 v176, v128, v129\n"
    "v_cvt_pk_bf16_f32 v177, v130, v131\n"
    "v_cvt_pk_bf16_f32 v178, v132, v133\n"
    "v_cvt_pk_bf16_f32 v179, v134, v135\n"
    "v_cvt_pk_bf16_f32 v180, v136, v137\n"
    "v_cvt_pk_bf16_f32 v181, v138, v139\n"
    "v_cvt_pk_bf16_f32 v182, v140, v141\n"
    "v_cvt_pk_bf16_f32 v183, v142, v143\n"
    "s_waitcnt vmcnt(0) lgkmcnt(0)\n"
    "s_barrier\n"
    "ds_read_b128 v[192:195], v255 offset:32768\n"
    "ds_read_b128 v[80:83], v78\n"
    "ds_read_b128 v[84:87], v79\n"
    "ds_read_b128 v[88:91], v77 offset:32768\n"
    "ds_read_b128 v[92:95], v77 offset:34816\n"
    "s_mov_b64 exec, s[30:31]\n"
    "ds_read_b128 v[64:67], v77 offset:33792\n"
    "ds_read_b128 v[68:71], v77 offset:34048\n"
    "s_mov_b64 exec, -1\n"
    "s_cmp_eq_u32 s36, 0\n"
    "s_cbranch_scc1 kc24_%=\n"
    "s_setprio 1\n"
    "s_and_b32 s21, s4, 3\n"
    "s_lshl_b32 s21, s21, 12\n"
    "s_add_u32 s23, s21, s89\n"
    "s_add_u32 s23, s23, 49152\n"
    "s_and_b32 s23, s23, 0x1ffff\n"
    "s_add_u32 m0, s21, 81920\n"
    "s_or_b32 s23, s23, 131072\n"
    "buffer_load_dwordx4 v255, s[76:79], s23 offen offset:0 sc1 lds\n"
    "buffer_load_dwordx4 v255, s[76:79], s23 offen offset:1024 sc1 lds\n"
    "buffer_load_dwordx4 v255, s[76:79], s23 offen offset:2048 sc1 lds\n"
    "buffer_load_dwordx4 v255, s[76:79], s23 offen offset:3072 sc1 lds\n"
    "s_and_b32 s21, s4, 3\n"
    "s_lshl_b32 s21, s21, 12\n"
    "s_add_u32 s23, s21, s89\n"
    "s_add_u32 s23, s23, 32768\n"
    "s_and_b32 s23, s23, 0x1ffff\n"
    "s_add_u32 m0, s21, 65536\n"
    "s_or_b32 s23, s23, 131072\n"
    "buffer_load_dwordx4 v255, s[76:79], s23 offen offset:0 sc1 lds\n"
    "buffer_load_dwordx4 v255, s[76:79], s23 offen offset:1024 sc1 lds\n"
    "buffer_load_dwordx4 v255, s[76:79], s23 offen offset:2048 sc1 lds\n"
    "buffer_load_dwordx4 v255, s[76:79], s23 offen offset:3072 sc1 lds\n"
    "kc24_%=:\n"
    "s_waitcnt lgkmcnt(5)\n"
    "v_mfma_f32_32x32x16_bf16 v[96:111], v[192:195], v[80:83], 0\n"
    "s_waitcnt lgkmcnt(4)\n"
    "v_mfma_f32_32x32x16_bf16 v[112:127], v[192:195], v[84:87], 0\n"
    "v_max_f32 v144, 0, v144\n"
    "v_max_f32 v145, 0, v145\n"
    "v_max_f32 v146, 0, v146\n"
    "v_max_f32 v147, 0, v147\n"
    "v_max_f32 v148, 0, v148\n"
    "v_max_f32 v149, 0, v149\n"
    "v_max_f32 v150, 0, v150\n"
    "v_max_f32 v151, 0, v151\n"
    "v_max_f32 v152, 0, v152\n"
    "v_max_f32 v153, 0, v153\n"
    "v_max_f32 v154, 0, v154\n"
    "v_max_f32 v155, 0, v155\n"
    "v_max3_f32 v78, v96, v97, v98\n"
    "v_max3_f32 v78, v78, v99, v100\n"
    "v_max3_f32 v78, v78, v101, v102\n"
    "v_max3_f32 v78, v78, v103, v104\n"
    "v_max3_f32 v78, v78, v105, v106\n"
    "v_max3_f32 v78, v78, v107, v108\n"
    "v_max3_f32 v78, v78, v109, v110\n"
    "v_max_f32 v78, v78, v111\n"
    "v_max3_f32 v79, v112, v113, v114\n"
    "v_max3_f32 v79, v79, v115, v116\n"
    "v_max3_f32 v79, v79, v117, v118\n"
    "v_max3_f32 v79, v79, v119, v120\n"
    "v_max3_f32 v79, v79, v121, v122\n"
    "v_max3_f32 v79, v79, v123, v124\n"
    "v_max3_f32 v79, v79, v125, v126\n"
    "v_max_f32 v79, v79, v127\n"
    "v_mov_b32 v192, v78\n"
    "s_nop 1\n"
    "v_permlane32_swap_b32 v78, v192\n"
    "v_max_f32 v78, v78, v192\n"
    "v_mov_b32 v193, v79\n"
    "s_nop 1\n"
    "v_permlane32_swap_b32 v79, v193\n"
    "v_max_f32 v79, v79, v193\n"
    "v_mov_b32 v192, 0xc2340000\n"
    "v_fma_f32 v78, v78, -0.5, v192\n"
    "v_fma_f32 v79, v79, -0.5, v192\n"
    "v_cvt_pk_bf16_f32 v208, v78, v205\n"
    "v_cvt_pk_bf16_f32 v212, v79, v205\n"
    "v_mov_b32 v192, -1\n"
    "v_mov_b32 v193, -1\n"
    "v_mov_b32 v194, -1\n"
    "v_mov_b32 v195, -1\n"
    "v_mfma_f32_32x32x16_bf16 v[96:111], v[204:207], v[208:211], 0\n"
    "v_mfma_f32_32x32x16_bf16 v[112:127], v[204:207], v[212:215], 0\n"
    "v_max_f32 v156, 0, v156\n"
    "v_max_f32 v157, 0, v157\n"
    "v_max_f32 v158, 0, v158\n"
    "v_max_f32 v159, 0, v159\n"
    "v_pk_mul_f32 v[144:145], v[144:145], v[160:161] op_sel:[0,0] op_sel_hi:[1,0]\n"
    "v_pk_mul_f32 v[146:147], v[146:147], v[160:161] op_sel:[0,0] op_sel_hi:[1,0]\n"
    "v_pk_mul_f32 v[148:149], v[148:149], v[160:161] op_sel:[0,0] op_sel_hi:[1,0]\n"
    "v_pk_mul_f32 v[150:151], v[150:151], v[160:161] op_sel:[0,0] op_sel_hi:[1,0]\n"
    "v_pk_mul_f32 v[152:153], v[152:153], v[160:161] op_sel:[0,0] op_sel_hi:[1,0]\n"
    "v_pk_mul_f32 v[154:155], v[154:155], v[160:161] op_sel:[0,0] op_sel_hi:[1,0]\n"
    "v_pk_mul_f32 v[156:157], v[156:157], v[160:161] op_sel:[0,0] op_sel_hi:[1,0]\n"
    "v_pk_mul_f32 v[158:159], v[158:159], v[160:161] op_sel:[0,0] op_sel_hi:[1,0]\n"
    "s_waitcnt lgkmcnt(0)\n"
    "v_mfma_f32_32x32x16_bf16 v[0:15], v[88:91], v[80:83], v[96:111]\n"
    "v_pk_mul_f32 v[144:145], v[144:145], v[144:145] op_sel:[0,0] op_sel_hi:[1,1]\n"
    "v_pk_mul_f32 v[146:147], v[146:147], v[146:147] op_sel:[0,0] op_sel_hi:[1,1]\n"
    "v_pk_mul_f32 v[148:149], v[148:149], v[148:149] op_sel:[0,0] op_sel_hi:[1,1]\n"
    "v_pk_mul_f32 v[150:151], v[150:151], v[150:151] op_sel:[0,0] op_sel_hi:[1,1]\n"
    "v_pk_mul_f32 v[152:153], v[152:153], v[152:153] op_sel:[0,0] op_sel_hi:[1,1]\n"
    "v_pk_mul_f32 v[154:155], v[154:155], v[154:155] op_sel:[0,0] op_sel_hi:[1,1]\n"
    "v_pk_mul_f32 v[156:157], v[156:157], v[156:157] op_sel:[0,0] op_sel_hi:[1,1]\n"
    "v_pk_mul_f32 v[158:159], v[158:159], v[158:159] op_sel:[0,0] op_sel_hi:[1,1]\n"
    "v_cvt_pk_bf16_f32 v184, v144, v145\n"
    "v_cvt_pk_bf16_f32 v185, v146, v147\n"
    "v_cvt_pk_bf16_f32 v186, v148, v149\n"
    "v_cvt_pk_bf16_f32 v187, v150, v151\n"
    "v_cvt_pk_bf16_f32 v188, v152, v153\n"
    "v_cvt_pk_bf16_f32 v189, v154, v155\n"
    "v_cvt_pk_bf16_f32 v190, v156, v157\n"
    "v_cvt_pk_bf16_f32 v191, v158, v159\n"
    "at7_%=:\n"
    "s_add_u32 s88, s36, 1\n"
    "s_cmp_eq_u32 s36, 0\n"
    "s_cbranch_scc0 k25_%=\n"
    "s_add_u32 s24, s39, 0\n"
    "s_add_u32 s21, s7, s24\n"
    "s_and_b32 s23, s21, 1\n"
    "s_mul_i32 s23, s23, 44\n"
    "s_lshr_b32 s21, s21, 1\n"
    "s_add_u32 s21, s21, s23\n"
    "s_lshl_b32 s21, s21, 10\n"
    "s_add_u32 s21, s21, s19\n"
    "s_cmp_lt_u32 s24, s90\n"
    "s_cselect_b32 s21, s21, s14\n"
    "s_add_u32 s23, s21, 22528\n"
    "buffer_store_dwordx4 v[176:179], v255, s[76:79], s21 offen sc1\n"
    "buffer_store_dwordx4 v[180:183], v255, s[76:79], s23 offen sc1\n"
    "k25_%=:\n"
    "s_nop 7\n"
    "v_exp_f32 v0, v0\n"
    "v_exp_f32 v1, v1\n"
    "v_smfmac_f32_32x32x32_bf16 v[48:63], v[72:75], v[24:31], v76\n"
    "v_exp_f32 v2, v2\n"
    "v_exp_f32 v3, v3\n"
    "v_exp_f32 v4, v4\n"
    "v_exp_f32 v5, v5\n"
    "v_mfma_f32_32x32x16_bf16 v[16:31], v[88:91], v[84:87], v[112:127]\n"
    "v_exp_f32 v6, v6\n"
    "v_exp_f32 v7, v7\n"
    "v_exp_f32 v8, v8\n"
    "v_exp_f32 v9, v9\n"
    "s_waitcnt lgkmcnt(0)\n"
    "v_smfmac_f32_32x32x32_bf16 v[32:47], v[64:67], v[0:7], v76\n"
    "v_exp_f32 v10, v10\n"
    "v_exp_f32 v11, v11\n"
    "v_exp_f32 v12, v12\n"
    "v_exp_f32 v13, v13\n"
    "v_exp_f32 v14, v14\n"
    "v_exp_f32 v15, v15\n"
    "v_exp_f32 v16, v16\n"
    "v_exp_f32 v17, v17\n"
    "v_smfmac_f32_32x32x32_bf16 v[32:47], v[68:71], v[8:15], v76\n"
    "v_exp_f32 v18, v18\n"
    "v_exp_f32 v19, v19\n"
    "v_exp_f32 v20, v20\n"
    "v_exp_f32 v21, v21\n"
    "v_mfma_f32_32x32x16_bf16 v[0:15], v[92:95], v[80:83], v[96:111]\n"
    "v_exp_f32 v22, v22\n"
    "v_exp_f32 v23, v23\n"
    "v_exp_f32 v24, v24\n"
    "v_exp_f32 v25, v25\n"
    "v_smfmac_f32_32x32x32_bf16 v[48:63], v[64:67], v[16:23], v76\n"
    "ds_read_b128 v[88:91], v77 offset:36864\n"
    "s_mov_b64 exec, s[30:31]\n"
    "ds_read_b128 v[64:67], v77 offset:35840\n"
    "ds_read_b128 v[72:75], v77 offset:36096\n"
    "s_mov_b64 exec, -1\n"
    "v_exp_f32 v26, v26\n"
    "v_exp_f32 v27, v27\n"
    "v_exp_f32 v28, v28\n"
    "v_exp_f32 v29, v29\n"
    "v_exp_f32 v30, v30\n"
    "v_exp_f32 v31, v31\n"
    "s_cmp_eq_u32 s36, 0\n"
    "s_cbranch_scc0 k26_%=\n"
    "s_add_u32 s24, s39, 1\n"
    "s_add_u32 s21, s7, s24\n"
    "s_and_b32 s23, s21, 1\n"
    "s_mul_i32 s23, s23, 44\n"
    "s_lshr_b32 s21, s21, 1\n"
    "s_add_u32 s21, s21, s23\n"
    "s_lshl_b32 s21, s21, 10\n"
    "s_add_u32 s21, s21, s19\n"
    "s_cmp_lt_u32 s24, s90\n"
    "s_cselect_b32 s21, s21, s14\n"
    "s_add_u32 s23, s21, 22528\n"
    "buffer_store_dwordx4 v[184:187], v255, s[76:79], s21 offen sc1\n"
    "buffer_store_dwordx4 v[188:191], v255, s[76:79], s23 offen sc1\n"
    "k26_%=:\n"
    "s_cmp_eq_u32 s36, 1\n"
    "s_cbranch_scc0 k27_%=\n"
    "s_waitcnt vmcnt(0)\n"
    "s_and_b32 s21, s4, 3\n"
    "s_lshl_b32 s21, s21, 2\n"
    "s_add_u32 s21, s21, 158720\n"
    "v_mov_b32 v78, s21\n"
    "v_mov_b32 v79, 1\n"
    "ds_write_b32 v78, v79\n"
    "s_and_b32 s21, s4, 3\n"
    "s_lshl_b32 s21, s21, 12\n"
    "s_add_u32 s23, s21, s89\n"
    "s_add_u32 s23, s23, 81920\n"
    "s_and_b32 s23, s23, 0x1ffff\n"
    "s_add_u32 m0, s21, 114688\n"
    "s_or_b32 s23, s23, 131072\n"
    "buffer_load_dwordx4 v255, s[76:79], s23 offen offset:0 sc1 lds\n"
    "buffer_load_dwordx4 v255, s[76:79], s23 offen offset:1024 sc1 lds\n"
    "buffer_load_dwordx4 v255, s[76:79], s23 offen offset:2048 sc1 lds\n"
    "buffer_load_dwordx4 v255, s[76:79], s23 offen offset:3072 sc1 lds\n"
    "k27_%=:\n"
    "v_exp_f32 v0, v0\n"
    "v_exp_f32 v1, v1\n"
    "v_smfmac_f32_32x32x32_bf16 v[48:63], v[68:71], v[24:31], v76\n"
    "v_exp_f32 v2, v2\n"
    "v_exp_f32 v3, v3\n"
    "v_exp_f32 v4, v4\n"
    "v_exp_f32 v5, v5\n"
    "v_mfma_f32_32x32x16_bf16 v[16:31], v[92:95], v[84:87], v[112:127]\n"
    "v_exp_f32 v6, v6\n"
    "v_exp_f32 v7, v7\n"
    "v_exp_f32 v8, v8\n"
    "v_exp_f32 v9, v9\n"
    "s_waitcnt lgkmcnt(0)\n"
    "v_smfmac_f32_32x32x32_bf16 v[32:47], v[64:67], v[0:7], v76\n"
    "v_exp_f32 v10, v10\n"
    "v_exp_f32 v11, v11\n"
    "v_exp_f32 v12, v12\n"
    "v_exp_f32 v13, v13\n"
    "v_exp_f32 v14, v14\n"
    "v_exp_f32 v15, v15\n"
    "v_exp_f32 v16, v16\n"
    "v_exp_f32 v17, v17\n"
    "v_smfmac_f32_32x32x32_bf16 v[32:47], v[72:75], v[8:15], v76\n"
    "v_exp_f32 v18, v18\n"
    "v_exp_f32 v19, v19\n"
    "v_exp_f32 v20, v20\n"
    "v_exp_f32 v21, v21\n"
    "v_mfma_f32_32x32x16_bf16 v[0:15], v[88:91], v[80:83], v[96:111]\n"
    "v_exp_f32 v22, v22\n"
    "v_exp_f32 v23, v23\n"
    "v_exp_f32 v24, v24\n"
    "v_exp_f32 v25, v25\n"
    "v_smfmac_f32_32x32x32_bf16 v[48:63], v[64:67], v[16:23], v76\n"
    "ds_read_b128 v[92:95], v77 offset:38912\n"
    "s_mov_b64 exec, s[30:31]\n"
    "ds_read_b128 v[64:67], v77 offset:37888\n"
    "ds_read_b128 v[68:71], v77 offset:38144\n"
    "s_mov_b64 exec, -1\n"
    "v_exp_f32 v26, v26\n"
    "v_exp_f32 v27, v27\n"
    "v_exp_f32 v28, v28\n"
    "v_exp_f32 v29, v29\n"
    "v_exp_f32 v30, v30\n"
    "v_exp_f32 v31, v31\n"
    "s_cmp_eq_u32 s36, 0\n"
    "s_cbranch_scc0 k28_%=\n"
    "buffer_store_dwordx4 v[192:195], v255, s[76:79], s20 offen sc1\n"
    "k28_%=:\n"
    "s_cmp_eq_u32 s36, 1\n"
    "s_cbranch_scc0 k29_%=\n"
    "s_and_b32 s21, s4, 3\n"
    "s_lshl_b32 s21, s21, 12\n"
    "s_add_u32 s23, s21, s89\n"
    "s_add_u32 s23, s23, 65536\n"
    "s_and_b32 s23, s23, 0x1ffff\n"
    "s_add_u32 m0, s21, 98304\n"
    "s_or_b32 s23, s23, 131072\n"
    "buffer_load_dwordx4 v255, s[76:79], s23 offen offset:0 sc1 lds\n"
    "buffer_load_dwordx4 v255, s[76:79], s23 offen offset:1024 sc1 lds\n"
    "buffer_load_dwordx4 v255, s[76:79], s23 offen offset:2048 sc1 lds\n"
    "buffer_load_dwordx4 v255, s[76:79], s23 offen offset:3072 sc1 lds\n"
    "k29_%=:\n"
    "v_exp_f32 v0, v0\n"
    "v_exp_f32 v1, v1\n"
    "v_smfmac_f32_32x32x32_bf16 v[48:63], v[72:75], v[24:31], v76\n"
    "v_exp_f32 v2, v2\n"
    "v_exp_f32 v3, v3\n"
    "v_exp_f32 v4, v4\n"
    "v_exp_f32 v5, v5\n"
    "v_mfma_f32_32x32x16_bf16 v[16:31], v[88:91], v[84:87], v[112:127]\n"
    "v_exp_f32 v6, v6\n"
    "v_exp_f32 v7, v7\n"
    "v_exp_f32 v8, v8\n"
    "v_exp_f32 v9, v9\n"
    "s_waitcnt lgkmcnt(0)\n"
    "v_smfmac_f32_32x32x32_bf16 v[32:47], v[64:67], v[0:7], v76\n"
    "v_exp_f32 v10, v10\n"
    "v_exp_f32 v11, v11\n"
    "v_exp_f32 v12, v12\n"
    "v_exp_f32 v13, v13\n"
    "v_exp_f32 v14, v14\n"
    "v_exp_f32 v15, v15\n"
    "v_exp_f32 v16, v16\n"
    "v_exp_f32 v17, v17\n"
    "v_smfmac_f32_32x32x32_bf16 v[32:47], v[68:71], v[8:15], v76\n"
    "v_exp_f32 v18, v18\n"
    "v_exp_f32 v19, v19\n"
    "v_exp_f32 v20, v20\n"
    "v_exp_f32 v21, v21\n"
    "v_mfma_f32_32x32x16_bf16 v[0:15], v[92:95], v[80:83], v[96:111]\n"
    "v_exp_f32 v22, v22\n"
    "v_exp_f32 v23, v23\n"
    "v_exp_f32 v24, v24\n"
    "v_exp_f32 v25, v25\n"
    "v_smfmac_f32_32x32x32_bf16 v[48:63], v[64:67], v[16:23], v76\n"
    "ds_read_b128 v[88:91], v77 offset:40960\n"
    "s_mov_b64 exec, s[30:31]\n"
    "ds_read_b128 v[64:67], v77 offset:39936\n"
    "ds_read_b128 v[72:75], v77 offset:40192\n"
    "s_mov_b64 exec, -1\n"
    "v_exp_f32 v26, v26\n"
    "v_exp_f32 v27, v27\n"
    "v_exp_f32 v28, v28\n"
    "v_exp_f32 v29, v29\n"
    "v_exp_f32 v30, v30\n"
    "v_exp_f32 v31, v31\n"
    "s_cmp_eq_u32 s36, 1\n"
    "s_cbranch_scc0 k30_%=\n"
    "s_and_b32 s21, s4, 3\n"
    "s_lshl_b32 s21, s21, 12\n"
    "s_add_u32 s23, s21, s89\n"
    "s_add_u32 s23, s23, 114688\n"
    "s_and_b32 s23, s23, 0x1ffff\n"
    "s_add_u32 m0, s21, 16384\n"
    "s_or_b32 s23, s23, 131072\n"
    "buffer_load_dwordx4 v255, s[76:79], s23 offen offset:0 sc1 lds\n"
    "buffer_load_dwordx4 v255, s[76:79], s23 offen offset:1024 sc1 lds\n"
    "buffer_load_dwordx4 v255, s[76:79], s23 offen offset:2048 sc1 lds\n"
    "buffer_load_dwordx4 v255, s[76:79], s23 offen offset:3072 sc1 lds\n"
    "k30_%=:\n"
    "s_cmp_lg_u32 s88, 1\n"
    "s_cbranch_scc1 hf31_%=\n"
    "s_getreg_b32 s21, hwreg(HW_REG_IB_STS)\n"
    "s_and_b32 s21, s21, 0xc000000f\n"
    "s_cbranch_scc1 hf31_%=\n"
    "s_waitcnt vmcnt(0)\n"
    "s_mov_b32 s88, 2\n"
    "s_mov_b64 exec, 1\n"
    "v_mov_b32 v78, s8\n"
    "v_mov_b32 v79, s9\n"
    "buffer_store_dwordx2 v[78:79], off, s[76:79], s34 sc1\n"
    "s_mov_b64 exec, -1\n"
    "hf31_%=:\n"
    "v_exp_f32 v0, v0\n"
    "v_exp_f32 v1, v1\n"
    "v_smfmac_f32_32x32x32_bf16 v[48:63], v[68:71], v[24:31], v76\n"
    "v_exp_f32 v2, v2\n"
    "v_exp_f32 v3, v3\n"
    "v_exp_f32 v4, v4\n"
    "v_exp_f32 v5, v5\n"
    "v_mfma_f32_32x32x16_bf16 v[16:31], v[92:95], v[84:87], v[112:127]\n"
    "v_exp_f32 v6, v6\n"
    "v_exp_f32 v7, v7\n"
    "v_exp_f32 v8, v8\n"
    "v_exp_f32 v9, v9\n"
    "s_waitcnt lgkmcnt(0)\n"
    "v_smfmac_f32_32x32x32_bf16 v[32:47], v[64:67], v[0:7], v76\n"
    "v_exp_f32 v10, v10\n"
    "v_exp_f32 v11, v11\n"
    "v_exp_f32 v12, v12\n"
    "v_exp_f32 v13, v13\n"
    "v_exp_f32 v14, v14\n"
    "v_exp_f32 v15, v15\n"
    "v_exp_f32 v16, v16\n"
    "v_exp_f32 v17, v17\n"
    "v_smfmac_f32_32x32x32_bf16 v[32:47], v[72:75], v[8:15], v76\n"
    "v_exp_f32 v18, v18\n"
    "v_exp_f32 v19, v19\n"
    "v_exp_f32 v20, v20\n"
    "v_exp_f32 v21, v21\n"
    "v_mfma_f32_32x32x16_bf16 v[0:15], v[88:91], v[80:83], v[96:111]\n"
    "v_exp_f32 v22, v22\n"
    "v_exp_f32 v23, v23\n"
    "v_exp_f32 v24, v24\n"
    "v_exp_f32 v25, v25\n"
    "v_smfmac_f32_32x32x32_bf16 v[48:63], v[64:67], v[16:23], v76\n"
    "ds_read_b128 v[92:95], v77 offset:43008\n"
    "s_mov_b64 exec, s[30:31]\n"
    "ds_read_b128 v[64:67], v77 offset:41984\n"
    "ds_read_b128 v[68:71], v77 offset:42240\n"
    "s_mov_b64 exec, -1\n"
    "v_exp_f32 v26, v26\n"
    "v_exp_f32 v27, v27\n"
    "v_exp_f32 v28, v28\n"
    "v_exp_f32 v29, v29\n"
    "v_exp_f32 v30, v30\n"
    "v_exp_f32 v31, v31\n"
    "s_cmp_eq_u32 s36, 1\n"
    "s_cbranch_scc0 k33_%=\n"
    "s_and_b32 s21, s4, 3\n"
    "s_lshl_b32 s21, s21, 12\n"
    "s_add_u32 s23, s21, s89\n"
    "s_add_u32 s23, s23, 98304\n"
    "s_and_b32 s23, s23, 0x1ffff\n"
    "s_add_u32 m0, s21, 0\n"
    "s_or_b32 s23, s23, 131072\n"
    "buffer_load_dwordx4 v255, s[76:79], s23 offen offset:0 sc1 lds\n"
    "buffer_load_dwordx4 v255, s[76:79], s23 offen offset:1024 sc1 lds\n"
    "buffer_load_dwordx4 v255, s[76:79], s23 offen offset:2048 sc1 lds\n"
    "buffer_load_dwordx4 v255, s[76:79], s23 offen offset:3072 sc1 lds\n"
    "k33_%=:\n"
    "s_cmp_lg_u32 s88, 1\n"
    "s_cbranch_scc1 hf34_%=\n"
    "s_getreg_b32 s21, hwreg(HW_REG_IB_STS)\n"
    "s_and_b32 s21, s21, 0xc000000f\n"
    "s_cbranch_scc1 hf34_%=\n"
    "s_waitcnt vmcnt(0)\n"
    "s_mov_b32 s88, 2\n"
    "s_mov_b64 exec, 1\n"
    "v_mov_b32 v78, s8\n"
    "v_mov_b32 v79, s9\n"
    "buffer_store_dwordx2 v[78:79], off, s[76:79], s34 sc1\n"
    "s_mov_b64 exec, -1\n"
    "hf34_%=:\n"
    "v_exp_f32 v0, v0\n"
    "v_exp_f32 v1, v1\n"
    "v_smfmac_f32_32x32x32_bf16 v[48:63], v[72:75], v[24:31], v76\n"
    "v_exp_f32 v2, v2\n"
    "v_exp_f32 v3, v3\n"
    "v_exp_f32 v4, v4\n"
    "v_exp_f32 v5, v5\n"
    "v_mfma_f32_32x32x16_bf16 v[16:31], v[88:91], v[84:87], v[112:127]\n"
    "v_exp_f32 v6, v6\n"
    "v_exp_f32 v7, v7\n"
    "v_exp_f32 v8, v8\n"
    "v_exp_f32 v9, v9\n"
    "s_waitcnt lgkmcnt(0)\n"
    "v_smfmac_f32_32x32x32_bf16 v[32:47], v[64:67], v[0:7], v76\n"
    "v_exp_f32 v10, v10\n"
    "v_exp_f32 v11, v11\n"
    "v_exp_f32 v12, v12\n"
    "v_exp_f32 v13, v13\n"
    "v_exp_f32 v14, v14\n"
    "v_exp_f32 v15, v15\n"
    "v_exp_f32 v16, v16\n"
    "v_exp_f32 v17, v17\n"
    "v_smfmac_f32_32x32x32_bf16 v[32:47], v[68:71], v[8:15], v76\n"
    "v_exp_f32 v18, v18\n"
    "v_exp_f32 v19, v19\n"
    "v_exp_f32 v20, v20\n"
    "v_exp_f32 v21, v21\n"
    "v_mfma_f32_32x32x16_bf16 v[0:15], v[92:95], v[80:83], v[96:111]\n"
    "v_exp_f32 v22, v22\n"
    "v_exp_f32 v23, v23\n"
    "v_exp_f32 v24, v24\n"
    "v_exp_f32 v25, v25\n"
    "v_smfmac_f32_32x32x32_bf16 v[48:63], v[64:67], v[16:23], v76\n"
    "ds_read_b128 v[88:91], v77 offset:45056\n"
    "s_mov_b64 exec, s[30:31]\n"
    "ds_read_b128 v[64:67], v77 offset:44032\n"
    "ds_read_b128 v[72:75], v77 offset:44288\n"
    "s_mov_b64 exec, -1\n"
    "v_exp_f32 v26, v26\n"
    "v_exp_f32 v27, v27\n"
    "v_exp_f32 v28, v28\n"
    "v_exp_f32 v29, v29\n"
    "v_exp_f32 v30, v30\n"
    "v_exp_f32 v31, v31\n"
    "s_cmp_eq_u32 s36, 1\n"
    "s_cbranch_scc0 k36_%=\n"
    "s_add_u32 s24, s39, 0\n"
    "s_add_u32 s21, s7, s24\n"
    "s_and_b32 s23, s21, 1\n"
    "s_mul_i32 s23, s23, 44\n"
    "s_lshr_b32 s21, s21, 1\n"
    "s_add_u32 s21, s21, s23\n"
    "s_lshl_b32 s21, s21, 10\n"
    "s_add_u32 s21, s21, s19\n"
    "s_cmp_lt_u32 s24, s90\n"
    "s_cselect_b32 s21, s21, s14\n"
    "s_add_u32 s23, s21, 22528\n"
    "buffer_store_dwordx4 v[176:179], v255, s[76:79], s21 offen sc1\n"
    "buffer_store_dwordx4 v[180:183], v255, s[76:79], s23 offen sc1\n"
    "k36_%=:\n"
    "s_cmp_lg_u32 s88, 1\n"
    "s_cbranch_scc1 hf37_%=\n"
    "s_getreg_b32 s21, hwreg(HW_REG_IB_STS)\n"
    "s_and_b32 s21, s21, 0xc000000f\n"
    "s_cbranch_scc1 hf37_%=\n"
    "s_waitcnt vmcnt(0)\n"
    "s_mov_b32 s88, 2\n"
    "s_mov_b64 exec, 1\n"
    "v_mov_b32 v78, s8\n"
    "v_mov_b32 v79, s9\n"
    "buffer_store_dwordx2 v[78:79], off, s[76:79], s34 sc1\n"
    "s_mov_b64 exec, -1\n"
    "hf37_%=:\n"
    "v_exp_f32 v0, v0\n"
    "v_exp_f32 v1, v1\n"
    "v_smfmac_f32_32x32x32_bf16 v[48:63], v[68:71], v[24:31], v76\n"
    "v_exp_f32 v2, v2\n"
    "v_exp_f32 v3, v3\n"
    "v_exp_f32 v4, v4\n"
    "v_exp_f32 v5, v5\n"
    "v_mfma_f32_32x32x16_bf16 v[16:31], v[92:95], v[84:87], v[112:127]\n"
    "v_exp_f32 v6, v6\n"
    "v_exp_f32 v7, v7\n"
    "v_exp_f32 v8, v8\n"
    "v_exp_f32 v9, v9\n"
    "s_waitcnt lgkmcnt(0)\n"
    "v_smfmac_f32_32x32x32_bf16 v[32:47], v[64:67], v[0:7], v76\n"
    "v_exp_f32 v10, v10\n"
    "v_exp_f32 v11, v11\n"
    "v_exp_f32 v12, v12\n"
    "v_exp_f32 v13, v13\n"
    "v_exp_f32 v14, v14\n"
    "v_exp_f32 v15, v15\n"
    "v_exp_f32 v16, v16\n"
    "v_exp_f32 v17, v17\n"
    "v_smfmac_f32_32x32x32_bf16 v[32:47], v[72:75], v[8:15], v76\n"
    "v_exp_f32 v18, v18\n"
    "v_exp_f32 v19, v19\n"
    "v_exp_f32 v20, v20\n"
    "v_exp_f32 v21, v21\n"
    "v_mfma_f32_32x32x16_bf16 v[0:15], v[88:91], v[80:83], v[96:111]\n"
    "v_exp_f32 v22, v22\n"
    "v_exp_f32 v23, v23\n"
    "v_exp_f32 v24, v24\n"
    "v_exp_f32 v25, v25\n"
    "v_smfmac_f32_32x32x32_bf16 v[48:63], v[64:67], v[16:23], v76\n"
    "ds_read_b128 v[92:95], v77 offset:47104\n"
    "s_mov_b64 exec, s[30:31]\n"
    "ds_read_b128 v[64:67], v77 offset:46080\n"
    "ds_read_b128 v[68:71], v77 offset:46336\n"
    "s_mov_b64 exec, -1\n"
    "v_exp_f32 v26, v26\n"
    "v_exp_f32 v27, v27\n"
    "v_exp_f32 v28, v28\n"
    "v_exp_f32 v29, v29\n"
    "v_exp_f32 v30, v30\n"
    "v_exp_f32 v31, v31\n"
    "s_cmp_eq_u32 s36, 1\n"
    "s_cbranch_scc0 k39_%=\n"
    "s_add_u32 s24, s39, 1\n"
    "s_add_u32 s21, s7, s24\n"
    "s_and_b32 s23, s21, 1\n"
    "s_mul_i32 s23, s23, 44\n"
    "s_lshr_b32 s21, s21, 1\n"
    "s_add_u32 s21, s21, s23\n"
    "s_lshl_b32 s21, s21, 10\n"
    "s_add_u32 s21, s21, s19\n"
    "s_cmp_lt_u32 s24, s90\n"
    "s_cselect_b32 s21, s21, s14\n"
    "s_add_u32 s23, s21, 22528\n"
    "buffer_store_dwordx4 v[184:187], v255, s[76:79], s21 offen sc1\n"
    "buffer_store_dwordx4 v[188:191], v255, s[76:79], s23 offen sc1\n"
    "k39_%=:\n"
    "s_cmp_lg_u32 s88, 1\n"
    "s_cbranch_scc1 hf40_%=\n"
    "s_waitcnt vmcnt(0)\n"
    "s_mov_b32 s88, 2\n"
    "s_mov_b64 exec, 1\n"
    "v_mov_b32 v78, s8\n"
    "v_mov_b32 v79, s9\n"
    "buffer_store_dwordx2 v[78:79], off, s[76:79], s34 sc1\n"
    "s_mov_b64 exec, -1\n"
    "hf40_%=:\n"
    "s_mov_b32 s24, 0\n"
    "sg42_%=:\n"
    "ds_read_b32 v78, v247 offset:0\n"
    "s_add_u32 s24, s24, 1\n"
    "s_waitcnt lgkmcnt(0)\n"
    "v_cmp_ne_u32 s[26:27], 1, v78\n"
    "s_cmp_gt_u32 s24, 0x100000\n"
    "s_cbranch_scc1 so43_%=\n"
    "s_cmp_eq_u64 s[26:27], 0\n"
    "s_cbranch_scc0 sg42_%=\n"
    "so43_%=:\n"
    "v_exp_f32 v0, v0\n"
    "v_exp_f32 v1, v1\n"
    "v_smfmac_f32_32x32x32_bf16 v[48:63], v[72:75], v[24:31], v76\n"
    "v_exp_f32 v2, v2\n"
    "v_exp_f32 v3, v3\n"
    "v_exp_f32 v4, v4\n"
    "v_exp_f32 v5, v5\n"
    "v_mfma_f32_32x32x16_bf16 v[16:31], v[88:91], v[84:87], v[112:127]\n"
    "v_exp_f32 v6, v6\n"
    "v_exp_f32 v7, v7\n"
    "v_exp_f32 v8, v8\n"
    "v_exp_f32 v9, v9\n"
    "s_waitcnt lgkmcnt(0)\n"
    "v_smfmac_f32_32x32x32_bf16 v[32:47], v[64:67], v[0:7], v76\n"
    "v_exp_f32 v10, v10\n"
    "v_exp_f32 v11, v11\n"
    "v_exp_f32 v12, v12\n"
    "v_exp_f32 v13, v13\n"
    "v_exp_f32 v14, v14\n"
    "v_exp_f32 v15, v15\n"
    "v_exp_f32 v16, v16\n"
    "v_exp_f32 v17, v17\n"
    "v_smfmac_f32_32x32x32_bf16 v[32:47], v[68:71], v[8:15], v76\n"
    "v_exp_f32 v18, v18\n"
    "v_exp_f32 v19, v19\n"
    "v_exp_f32 v20, v20\n"
    "v_exp_f32 v21, v21\n"
    "v_mfma_f32_32x32x16_bf16 v[0:15], v[92:95], v[80:83], v[96:111]\n"
    "v_exp_f32 v22, v22\n"
    "v_exp_f32 v23, v23\n"
    "v_exp_f32 v24, v24\n"
    "v_exp_f32 v25, v25\n"
    "v_smfmac_f32_32x32x32_bf16 v[48:63], v[64:67], v[16:23], v76\n"
    "ds_read_b128 v[88:91], v250 offset:0\n"
    "s_mov_b64 exec, s[30:31]\n"
    "ds_read_b128 v[64:67], v77 offset:48128\n"
    "ds_read_b128 v[72:75], v77 offset:48384\n"
    "s_mov_b64 exec, -1\n"
    "v_exp_f32 v26, v26\n"
    "v_exp_f32 v27, v27\n"
    "v_exp_f32 v28, v28\n"
    "v_exp_f32 v29, v29\n"
    "v_exp_f32 v30, v30\n"
    "v_exp_f32 v31, v31\n"
    "s_cmp_eq_u32 s36, 1\n"
    "s_cbranch_scc0 k44_%=\n"
    "buffer_store_dwordx4 v[192:195], v255, s[76:79], s20 offen sc1\n"
    "k44_%=:\n"
    "v_exp_f32 v0, v0\n"
    "v_exp_f32 v1, v1\n"
    "v_smfmac_f32_32x32x32_bf16 v[48:63], v[68:71], v[24:31], v76\n"
    "v_exp_f32 v2, v2\n"
    "v_exp_f32 v3, v3\n"
    "v_exp_f32 v4, v4\n"
    "v_exp_f32 v5, v5\n"
    "v_mfma_f32_32x32x16_bf16 v[16:31], v[92:95], v[84:87], v[112:127]\n"
    "v_exp_f32 v6, v6\n"
    "v_exp_f32 v7, v7\n"
    "v_exp_f32 v8, v8\n"
    "v_exp_f32 v9, v9\n"
    "s_waitcnt lgkmcnt(0)\n"
    "v_smfmac_f32_32x32x32_bf16 v[32:47], v[64:67], v[0:7], v76\n"
    "v_exp_f32 v10, v10\n"
    "v_exp_f32 v11, v11\n"
    "v_exp_f32 v12, v12\n"
    "v_exp_f32 v13, v13\n"
    "v_exp_f32 v14, v14\n"
    "v_exp_f32 v15, v15\n"
    "v_exp_f32 v16, v16\n"
    "v_exp_f32 v17, v17\n"
    "v_smfmac_f32_32x32x32_bf16 v[32:47], v[72:75], v[8:15], v76\n"
    "v_exp_f32 v18, v18\n"
    "v_exp_f32 v19, v19\n"
    "v_exp_f32 v20, v20\n"
    "v_exp_f32 v21, v21\n"
    "v_mfma_f32_32x32x16_bf16 v[0:15], v[88:91], v[80:83], v[96:111]\n"
    "v_exp_f32 v22, v22\n"
    "v_exp_f32 v23, v23\n"
    "v_exp_f32 v24, v24\n"
    "v_exp_f32 v25, v25\n"
    "v_smfmac_f32_32x32x32_bf16 v[48:63], v[64:67], v[16:23], v76\n"
    "ds_read_b128 v[92:95], v250 offset:2048\n"
    "s_mov_b64 exec, s[30:31]\n"
    "ds_read_b128 v[64:67], v250 offset:1024\n"
    "ds_read_b128 v[68:71], v250 offset:1280\n"
    "s_mov_b64 exec, -1\n"
    "v_exp_f32 v26, v26\n"
    "v_exp_f32 v27, v27\n"
    "v_exp_f32 v28, v28\n"
    "v_exp_f32 v29, v29\n"
    "v_exp_f32 v30, v30\n"
    "v_exp_f32 v31, v31\n"
    "v_exp_f32 v0, v0\n"
    "v_exp_f32 v1, v1\n"
    "v_smfmac_f32_32x32x32_bf16 v[48:63], v[72:75], v[24:31], v76\n"
    "v_exp_f32 v2, v2\n"
    "v_exp_f32 v3, v3\n"
    "v_exp_f32 v4, v4\n"
    "v_exp_f32 v5, v5\n"
    "v_mfma_f32_32x32x16_bf16 v[16:31], v[88:91], v[84:87], v[112:127]\n"
    "v_exp_f32 v6, v6\n"
    "v_exp_f32 v7, v7\n"
    "v_exp_f32 v8, v8\n"
    "v_exp_f32 v9, v9\n"
    "s_waitcnt lgkmcnt(0)\n"
    "v_smfmac_f32_32x32x32_bf16 v[32:47], v[64:67], v[0:7], v76\n"
    "v_exp_f32 v10, v10\n"
    "v_exp_f32 v11, v11\n"
    "v_exp_f32 v12, v12\n"
    "v_exp_f32 v13, v13\n"
    "v_exp_f32 v14, v14\n"
    "v_exp_f32 v15, v15\n"
    "v_exp_f32 v16, v16\n"
    "v_exp_f32 v17, v17\n"
    "v_smfmac_f32_32x32x32_bf16 v[32:47], v[68:71], v[8:15], v76\n"
    "v_exp_f32 v18, v18\n"
    "v_exp_f32 v19, v19\n"
    "v_exp_f32 v20, v20\n"
    "v_exp_f32 v21, v21\n"
    "v_mfma_f32_32x32x16_bf16 v[0:15], v[92:95], v[80:83], v[96:111]\n"
    "v_exp_f32 v22, v22\n"
    "v_exp_f32 v23, v23\n"
    "v_exp_f32 v24, v24\n"
    "v_exp_f32 v25, v25\n"
    "v_smfmac_f32_32x32x32_bf16 v[48:63], v[64:67], v[16:23], v76\n"
    "ds_read_b128 v[88:91], v250 offset:4096\n"
    "s_mov_b64 exec, s[30:31]\n"
    "ds_read_b128 v[64:67], v250 offset:3072\n"
    "ds_read_b128 v[72:75], v250 offset:3328\n"
    "s_mov_b64 exec, -1\n"
    "v_exp_f32 v26, v26\n"
    "v_exp_f32 v27, v27\n"
    "v_exp_f32 v28, v28\n"
    "v_exp_f32 v29, v29\n"
    "v_exp_f32 v30, v30\n"
    "v_exp_f32 v31, v31\n"
    "v_exp_f32 v0, v0\n"
    "v_exp_f32 v1, v1\n"
    "v_smfmac_f32_32x32x32_bf16 v[48:63], v[68:71], v[24:31], v76\n"
    "v_exp_f32 v2, v2\n"
    "v_exp_f32 v3, v3\n"
    "v_exp_f32 v4, v4\n"
    "v_exp_f32 v5, v5\n"
    "v_mfma_f32_32x32x16_bf16 v[16:31], v[92:95], v[84:87], v[112:127]\n"
    "v_exp_f32 v6, v6\n"
    "v_exp_f32 v7, v7\n"
    "v_exp_f32 v8, v8\n"
    "v_exp_f32 v9, v9\n"
    "s_waitcnt lgkmcnt(0)\n"
    "v_smfmac_f32_32x32x32_bf16 v[32:47], v[64:67], v[0:7], v76\n"
    "v_exp_f32 v10, v10\n"
    "v_exp_f32 v11, v11\n"
    "v_exp_f32 v12, v12\n"
    "v_exp_f32 v13, v13\n"
    "v_exp_f32 v14, v14\n"
    "v_exp_f32 v15, v15\n"
    "v_exp_f32 v16, v16\n"
    "v_exp_f32 v17, v17\n"
    "v_smfmac_f32_32x32x32_bf16 v[32:47], v[72:75], v[8:15], v76\n"
    "v_exp_f32 v18, v18\n"
    "v_exp_f32 v19, v19\n"
    "v_exp_f32 v20, v20\n"
    "v_exp_f32 v21, v21\n"
    "v_mfma_f32_32x32x16_bf16 v[0:15], v[88:91], v[80:83], v[96:111]\n"
    "v_exp_f32 v22, v22\n"
    "v_exp_f32 v23, v23\n"
    "v_exp_f32 v24, v24\n"
    "v_exp_f32 v25, v25\n"
    "v_smfmac_f32_32x32x32_bf16 v[48:63], v[64:67], v[16:23], v76\n"
    "ds_read_b128 v[92:95], v250 offset:6144\n"
    "s_mov_b64 exec, s[30:31]\n"
    "ds_read_b128 v[64:67], v250 offset:5120\n"
    "ds_read_b128 v[68:71], v250 offset:5376\n"
    "s_mov_b64 exec, -1\n"
    "v_exp_f32 v26, v26\n"
    "v_exp_f32 v27, v27\n"
    "v_exp_f32 v28, v28\n"
    "v_exp_f32 v29, v29\n"
    "v_exp_f32 v30, v30\n"
    "v_exp_f32 v31, v31\n"
    "s_cmp_eq_u32 s36, 1\n"
    "s_cbranch_scc0 k45_%=\n"
    "s_waitcnt vmcnt(0)\n"
    "s_and_b32 s21, s4, 3\n"
    "s_lshl_b32 s21, s21, 2\n"
    "s_add_u32 s21, s21, 158736\n"
    "v_mov_b32 v78, s21\n"
    "v_mov_b32 v79, 1\n"
    "ds_write_b32 v78, v79\n"
    "s_mov_b64 exec, 1\n"
    "v_mov_b32 v78, s8\n"
    "v_mov_b32 v79, s9\n"
    "buffer_store_dwordx2 v[78:79], off, s[76:79], s34 sc1\n"
    "s_mov_b64 exec, -1\n"
    "k45_%=:\n"
    "v_exp_f32 v0, v0\n"
    "v_exp_f32 v1, v1\n"
    "v_smfmac_f32_32x32x32_bf16 v[48:63], v[72:75], v[24:31], v76\n"
    "v_exp_f32 v2, v2\n"
    "v_exp_f32 v3, v3\n"
    "v_exp_f32 v4, v4\n"
    "v_exp_f32 v5, v5\n"
    "v_mfma_f32_32x32x16_bf16 v[16:31], v[88:91], v[84:87], v[112:127]\n"
    "v_exp_f32 v6, v6\n"
    "v_exp_f32 v7, v7\n"
    "v_exp_f32 v8, v8\n"
    "v_exp_f32 v9, v9\n"
    "s_waitcnt lgkmcnt(0)\n"
    "v_smfmac_f32_32x32x32_bf16 v[32:47], v[64:67], v[0:7], v76\n"
    "v_exp_f32 v10, v10\n"
    "v_exp_f32 v11, v11\n"
    "v_exp_f32 v12, v12\n"
    "v_exp_f32 v13, v13\n"
    "v_exp_f32 v14, v14\n"
    "v_exp_f32 v15, v15\n"
    "v_exp_f32 v16, v16\n"
    "v_exp_f32 v17, v17\n"
    "v_smfmac_f32_32x32x32_bf16 v[32:47], v[68:71], v[8:15], v76\n"
    "v_exp_f32 v18, v18\n"
    "v_exp_f32 v19, v19\n"
    "v_exp_f32 v20, v20\n"
    "v_exp_f32 v21, v21\n"
    "v_mfma_f32_32x32x16_bf16 v[0:15], v[92:95], v[80:83], v[96:111]\n"
    "v_exp_f32 v22, v22\n"
    "v_exp_f32 v23, v23\n"
    "v_exp_f32 v24, v24\n"
    "v_exp_f32 v25, v25\n"
    "v_smfmac_f32_32x32x32_bf16 v[48:63], v[64:67], v[16:23], v76\n"
    "ds_read_b128 v[88:91], v250 offset:8192\n"
    "s_mov_b64 exec, s[30:31]\n"
    "ds_read_b128 v[64:67], v250 offset:7168\n"
    "ds_read_b128 v[72:75], v250 offset:7424\n"
    "s_mov_b64 exec, -1\n"
    "v_exp_f32 v26, v26\n"
    "v_exp_f32 v27, v27\n"
    "v_exp_f32 v28, v28\n"
    "v_exp_f32 v29, v29\n"
    "v_exp_f32 v30, v30\n"
    "v_exp_f32 v31, v31\n"
    "s_cmp_eq_u32 s36, 1\n"
    "s_cbranch_scc0 k47_%=\n"
    "s_and_b32 s21, s5, 7\n"
    "s_mul_i32 s21, s21, 236\n"
    "s_and_b32 s23, s4, 3\n"
    "s_lshl_b32 s23, s23, 6\n"
    "v_add_u32 v78, s23, v254\n"
    "v_min_u32 v78, 235, v78\n"
    "v_add_u32 v78, s21, v78\n"
    "v_lshlrev_b32 v78, 10, v78\n"
    "s_lshl_b32 s21, s6, 7\n"
    "s_mov_b32 m0, 157696\n"
    "v_subrev_u32 v79, 1409024, v78\n"
    "buffer_load_dword v78, s[68:71], s21 offen lds\n"
    "buffer_load_dword v79, s[56:59], s21 offen lds\n"
    "k47_%=:\n"
    "v_exp_f32 v0, v0\n"
    "v_exp_f32 v1, v1\n"
    "v_smfmac_f32_32x32x32_bf16 v[48:63], v[68:71], v[24:31], v76\n"
    "v_exp_f32 v2, v2\n"
    "v_exp_f32 v3, v3\n"
    "v_exp_f32 v4, v4\n"
    "v_exp_f32 v5, v5\n"
    "v_mfma_f32_32x32x16_bf16 v[16:31], v[92:95], v[84:87], v[112:127]\n"
    "v_exp_f32 v6, v6\n"
    "v_exp_f32 v7, v7\n"
    "v_exp_f32 v8, v8\n"
    "v_exp_f32 v9, v9\n"
    "s_waitcnt lgkmcnt(0)\n"
    "v_smfmac_f32_32x32x32_bf16 v[32:47], v[64:67], v[0:7], v76\n"
    "v_exp_f32 v10, v10\n"
    "v_exp_f32 v11, v11\n"
    "v_exp_f32 v12, v12\n"
    "v_exp_f32 v13, v13\n"
    "v_exp_f32 v14, v14\n"
    "v_exp_f32 v15, v15\n"
    "v_exp_f32 v16, v16\n"
    "v_exp_f32 v17, v17\n"
    "v_smfmac_f32_32x32x32_bf16 v[32:47], v[72:75], v[8:15], v76\n"
    "v_exp_f32 v18, v18\n"
    "v_exp_f32 v19, v19\n"
    "v_exp_f32 v20, v20\n"
    "v_exp_f32 v21, v21\n"
    "v_mfma_f32_32x32x16_bf16 v[0:15], v[88:91], v[80:83], v[96:111]\n"
    "v_exp_f32 v22, v22\n"
    "v_exp_f32 v23, v23\n"
    "v_exp_f32 v24, v24\n"
    "v_exp_f32 v25, v25\n"
    "v_smfmac_f32_32x32x32_bf16 v[48:63], v[64:67], v[16:23], v76\n"
    "ds_read_b128 v[92:95], v250 offset:10240\n"
    "s_mov_b64 exec, s[30:31]\n"
    "ds_read_b128 v[64:67], v250 offset:9216\n"
    "ds_read_b128 v[68:71], v250 offset:9472\n"
    "s_mov_b64 exec, -1\n"
    "v_exp_f32 v26, v26\n"
    "v_exp_f32 v27, v27\n"
    "v_exp_f32 v28, v28\n"
    "v_exp_f32 v29, v29\n"
    "v_exp_f32 v30, v30\n"
    "v_exp_f32 v31, v31\n"
    "v_exp_f32 v0, v0\n"
    "v_exp_f32 v1, v1\n"
    "v_smfmac_f32_32x32x32_bf16 v[48:63], v[72:75], v[24:31], v76\n"
    "v_exp_f32 v2, v2\n"
    "v_exp_f32 v3, v3\n"
    "v_exp_f32 v4, v4\n"
    "v_exp_f32 v5, v5\n"
    "v_mfma_f32_32x32x16_bf16 v[16:31], v[88:91], v[84:87], v[112:127]\n"
    "v_exp_f32 v6, v6\n"
    "v_exp_f32 v7, v7\n"
    "v_exp_f32 v8, v8\n"
    "v_exp_f32 v9, v9\n"
    "s_waitcnt lgkmcnt(0)\n"
    "v_smfmac_f32_32x32x32_bf16 v[32:47], v[64:67], v[0:7], v76\n"
    "v_exp_f32 v10, v10\n"
    "v_exp_f32 v11, v11\n"
    "v_exp_f32 v12, v12\n"
    "v_exp_f32 v13, v13\n"
    "v_exp_f32 v14, v14\n"
    "v_exp_f32 v15, v15\n"
    "v_exp_f32 v16, v16\n"
    "v_exp_f32 v17, v17\n"
    "v_smfmac_f32_32x32x32_bf16 v[32:47], v[68:71], v[8:15], v76\n"
    "v_exp_f32 v18, v18\n"
    "v_exp_f32 v19, v19\n"
    "v_exp_f32 v20, v20\n"
    "v_exp_f32 v21, v21\n"
    "v_mfma_f32_32x32x16_bf16 v[0:15], v[92:95], v[80:83], v[96:111]\n"
    "v_exp_f32 v22, v22\n"
    "v_exp_f32 v23, v23\n"
    "v_exp_f32 v24, v24\n"
    "v_exp_f32 v25, v25\n"
    "v_smfmac_f32_32x32x32_bf16 v[48:63], v[64:67], v[16:23], v76\n"
    "ds_read_b128 v[88:91], v250 offset:12288\n"
    "s_mov_b64 exec, s[30:31]\n"
    "ds_read_b128 v[64:67], v250 offset:11264\n"
    "ds_read_b128 v[72:75], v250 offset:11520\n"
    "s_mov_b64 exec, -1\n"
    "v_exp_f32 v26, v26\n"
    "v_exp_f32 v27, v27\n"
    "v_exp_f32 v28, v28\n"
    "v_exp_f32 v29, v29\n"
    "v_exp_f32 v30, v30\n"
    "v_exp_f32 v31, v31\n"
    "v_exp_f32 v0, v0\n"
    "v_exp_f32 v1, v1\n"
    "v_smfmac_f32_32x32x32_bf16 v[48:63], v[68:71], v[24:31], v76\n"
    "v_exp_f32 v2, v2\n"
    "v_exp_f32 v3, v3\n"
    "v_exp_f32 v4, v4\n"
    "v_exp_f32 v5, v5\n"
    "v_mfma_f32_32x32x16_bf16 v[16:31], v[92:95], v[84:87], v[112:127]\n"
    "v_exp_f32 v6, v6\n"
    "v_exp_f32 v7, v7\n"
    "v_exp_f32 v8, v8\n"
    "v_exp_f32 v9, v9\n"
    "s_waitcnt lgkmcnt(0)\n"
    "v_smfmac_f32_32x32x32_bf16 v[32:47], v[64:67], v[0:7], v76\n"
    "v_exp_f32 v10, v10\n"
    "v_exp_f32 v11, v11\n"
    "v_exp_f32 v12, v12\n"
    "v_exp_f32 v13, v13\n"
    "v_exp_f32 v14, v14\n"
    "v_exp_f32 v15, v15\n"
    "v_exp_f32 v16, v16\n"
    "v_exp_f32 v17, v17\n"
    "v_smfmac_f32_32x32x32_bf16 v[32:47], v[72:75], v[8:15], v76\n"
    "v_exp_f32 v18, v18\n"
    "v_exp_f32 v19, v19\n"
    "v_exp_f32 v20, v20\n"
    "v_exp_f32 v21, v21\n"
    "v_mfma_f32_32x32x16_bf16 v[0:15], v[88:91], v[80:83], v[96:111]\n"
    "v_exp_f32 v22, v22\n"
    "v_exp_f32 v23, v23\n"
    "v_exp_f32 v24, v24\n"
    "v_exp_f32 v25, v25\n"
    "v_smfmac_f32_32x32x32_bf16 v[48:63], v[64:67], v[16:23], v76\n"
    "ds_read_b128 v[92:95], v250 offset:14336\n"
    "s_mov_b64 exec, s[30:31]\n"
    "ds_read_b128 v[64:67], v250 offset:13312\n"
    "ds_read_b128 v[68:71], v250 offset:13568\n"
    "s_mov_b64 exec, -1\n"
    "v_exp_f32 v26, v26\n"
    "v_exp_f32 v27, v27\n"
    "v_exp_f32 v28, v28\n"
    "v_exp_f32 v29, v29\n"
    "v_exp_f32 v30, v30\n"
    "v_exp_f32 v31, v31\n"
    "s_mov_b32 s24, 0\n"
    "sg48_%=:\n"
    "ds_read_b32 v78, v247 offset:16\n"
    "s_add_u32 s24, s24, 1\n"
    "s_waitcnt lgkmcnt(0)\n"
    "v_cmp_ne_u32 s[26:27], 1, v78\n"
    "s_cmp_gt_u32 s24, 0x100000\n"
    "s_cbranch_scc1 so49_%=\n"
    "s_cmp_eq_u64 s[26:27], 0\n"
    "s_cbranch_scc0 sg48_%=\n"
    "so49_%=:\n"
    "v_exp_f32 v0, v0\n"
    "v_exp_f32 v1, v1\n"
    "v_smfmac_f32_32x32x32_bf16 v[48:63], v[72:75], v[24:31], v76\n"
    "v_exp_f32 v2, v2\n"
    "v_exp_f32 v3, v3\n"
    "v_exp_f32 v4, v4\n"
    "v_exp_f32 v5, v5\n"
    "v_mfma_f32_32x32x16_bf16 v[16:31], v[88:91], v[84:87], v[112:127]\n"
    "v_exp_f32 v6, v6\n"
    "v_exp_f32 v7, v7\n"
    "v_exp_f32 v8, v8\n"
    "v_exp_f32 v9, v9\n"
    "s_waitcnt lgkmcnt(0)\n"
    "v_smfmac_f32_32x32x32_bf16 v[32:47], v[64:67], v[0:7], v76\n"
    "v_exp_f32 v10, v10\n"
    "v_exp_f32 v11, v11\n"
    "v_exp_f32 v12, v12\n"
    "v_exp_f32 v13, v13\n"
    "v_exp_f32 v14, v14\n"
    "v_exp_f32 v15, v15\n"
    "v_exp_f32 v16, v16\n"
    "v_exp_f32 v17, v17\n"
    "v_smfmac_f32_32x32x32_bf16 v[32:47], v[68:71], v[8:15], v76\n"
    "v_exp_f32 v18, v18\n"
    "v_exp_f32 v19, v19\n"
    "v_exp_f32 v20, v20\n"
    "v_exp_f32 v21, v21\n"
    "v_mfma_f32_32x32x16_bf16 v[0:15], v[92:95], v[80:83], v[96:111]\n"
    "v_exp_f32 v22, v22\n"
    "v_exp_f32 v23, v23\n"
    "v_exp_f32 v24, v24\n"
    "v_exp_f32 v25, v25\n"
    "v_smfmac_f32_32x32x32_bf16 v[48:63], v[64:67], v[16:23], v76\n"
    "ds_read_b128 v[88:91], v250 offset:32768\n"
    "s_mov_b64 exec, s[30:31]\n"
    "ds_read_b128 v[64:67], v250 offset:15360\n"
    "ds_read_b128 v[72:75], v250 offset:15616\n"
    "s_mov_b64 exec, -1\n"
    "v_exp_f32 v26, v26\n"
    "v_exp_f32 v27, v27\n"
    "v_exp_f32 v28, v28\n"
    "v_exp_f32 v29, v29\n"
    "v_exp_f32 v30, v30\n"
    "v_exp_f32 v31, v31\n"
    "v_exp_f32 v0, v0\n"
    "v_exp_f32 v1, v1\n"
    "v_smfmac_f32_32x32x32_bf16 v[48:63], v[68:71], v[24:31], v76\n"
    "v_exp_f32 v2, v2\n"
    "v_exp_f32 v3, v3\n"
    "v_exp_f32 v4, v4\n"
    "v_exp_f32 v5, v5\n"
    "v_mfma_f32_32x32x16_bf16 v[16:31], v[92:95], v[84:87], v[112:127]\n"
    "v_exp_f32 v6, v6\n"
    "v_exp_f32 v7, v7\n"
    "v_exp_f32 v8, v8\n"
    "v_exp_f32 v9, v9\n"
    "s_waitcnt lgkmcnt(0)\n"
    "v_smfmac_f32_32x32x32_bf16 v[32:47], v[64:67], v[0:7], v76\n"
    "v_exp_f32 v10, v10\n"
    "v_exp_f32 v11, v11\n"
    "v_exp_f32 v12, v12\n"
    "v_exp_f32 v13, v13\n"
    "v_exp_f32 v14, v14\n"
    "v_exp_f32 v15, v15\n"
    "v_exp_f32 v16, v16\n"
    "v_exp_f32 v17, v17\n"
    "v_smfmac_f32_32x32x32_bf16 v[32:47], v[72:75], v[8:15], v76\n"
    "v_exp_f32 v18, v18\n"
    "v_exp_f32 v19, v19\n"
    "v_exp_f32 v20, v20\n"
    "v_exp_f32 v21, v21\n"
    "v_mfma_f32_32x32x16_bf16 v[0:15], v[88:91], v[80:83], v[96:111]\n"
    "v_exp_f32 v22, v22\n"
    "v_exp_f32 v23, v23\n"
    "v_exp_f32 v24, v24\n"
    "v_exp_f32 v25, v25\n"
    "v_smfmac_f32_32x32x32_bf16 v[48:63], v[64:67], v[16:23], v76\n"
    "ds_read_b128 v[92:95], v250 offset:34816\n"
    "s_mov_b64 exec, s[30:31]\n"
    "ds_read_b128 v[64:67], v250 offset:33792\n"
    "ds_read_b128 v[68:71], v250 offset:34048\n"
    "s_mov_b64 exec, -1\n"
    "v_exp_f32 v26, v26\n"
    "v_exp_f32 v27, v27\n"
    "v_exp_f32 v28, v28\n"
    "v_exp_f32 v29, v29\n"
    "v_exp_f32 v30, v30\n"
    "v_exp_f32 v31, v31\n"
    "v_lshlrev_b32 v128, 3, v254\n"
    "s_mov_b32 s24, 0\n"
    "sp51_%=:\n"
    "buffer_load_dwordx2 v[78:79], v128, s[76:79], s35 offen sc1\n"
    "s_add_u32 s24, s24, 1\n"
    "s_waitcnt vmcnt(0)\n"
    "v_cmp_ne_u64 s[26:27], v[78:79], s[8:9]\n"
    "s_cmp_eq_u64 s[26:27], 0\n"
    "s_cbranch_scc1 ok50_%=\n"
    "s_sleep 2\n"
    "s_cmp_gt_u32 s24, 0x40000\n"
    "s_cbranch_scc0 sp51_%=\n"
    "ok50_%=:\n"
    "s_cmp_eq_u32 s36, 0\n"
    "s_cbranch_scc0 k52_%=\n"
    "s_and_b32 s21, s4, 3\n"
    "s_lshl_b32 s21, s21, 2\n"
    "s_add_u32 s21, s21, 158768\n"
    "v_mov_b32 v78, s21\n"
    "v_mov_b32 v79, 1\n"
    "ds_write_b32 v78, v79\n"
    "k52_%=:\n"
    "v_exp_f32 v0, v0\n"
    "v_exp_f32 v1, v1\n"
    "v_smfmac_f32_32x32x32_bf16 v[48:63], v[72:75], v[24:31], v76\n"
    "v_exp_f32 v2, v2\n"
    "v_exp_f32 v3, v3\n"
    "v_exp_f32 v4, v4\n"
    "v_exp_f32 v5, v5\n"
    "v_mfma_f32_32x32x16_bf16 v[16:31], v[88:91], v[84:87], v[112:127]\n"
    "v_exp_f32 v6, v6\n"
    "v_exp_f32 v7, v7\n"
    "v_exp_f32 v8, v8\n"
    "v_exp_f32 v9, v9\n"
    "s_waitcnt lgkmcnt(0)\n"
    "v_smfmac_f32_32x32x32_bf16 v[32:47], v[64:67], v[0:7], v76\n"
    "v_exp_f32 v10, v10\n"
    "v_exp_f32 v11, v11\n"
    "v_exp_f32 v12, v12\n"
    "v_exp_f32 v13, v13\n"
    "v_exp_f32 v14, v14\n"
    "v_exp_f32 v15, v15\n"
    "v_exp_f32 v16, v16\n"
    "v_exp_f32 v17, v17\n"
    "v_smfmac_f32_32x32x32_bf16 v[32:47], v[68:71], v[8:15], v76\n"
    "v_exp_f32 v18, v18\n"
    "v_exp_f32 v19, v19\n"
    "v_exp_f32 v20, v20\n"
    "v_exp_f32 v21, v21\n"
    "v_mfma_f32_32x32x16_bf16 v[0:15], v[92:95], v[80:83], v[96:111]\n"
    "v_exp_f32 v22, v22\n"
    "v_exp_f32 v23, v23\n"
    "v_exp_f32 v24, v24\n"
    "v_exp_f32 v25, v25\n"
    "v_smfmac_f32_32x32x32_bf16 v[48:63], v[64:67], v[16:23], v76\n"
    "ds_read_b128 v[88:91], v250 offset:36864\n"
    "s_mov_b64 exec, s[30:31]\n"
    "ds_read_b128 v[64:67], v250 offset:35840\n"
    "ds_read_b128 v[72:75], v250 offset:36096\n"
    "s_mov_b64 exec, -1\n"
    "v_exp_f32 v26, v26\n"
    "v_exp_f32 v27, v27\n"
    "v_exp_f32 v28, v28\n"
    "v_exp_f32 v29, v29\n"
    "v_exp_f32 v30, v30\n"
    "v_exp_f32 v31, v31\n"
    "buffer_load_dwordx4 v[128:131], v255, s[76:79], s86 offen offset:0 sc1 nt\n"
    "buffer_load_dwordx4 v[132:135], v255, s[76:79], s86 offen offset:1024 sc1 nt\n"
    "buffer_load_dwordx4 v[136:139], v255, s[76:79], s86 offen offset:2048 sc1 nt\n"
    "v_exp_f32 v0, v0\n"
    "v_exp_f32 v1, v1\n"
    "v_smfmac_f32_32x32x32_bf16 v[48:63], v[68:71], v[24:31], v76\n"
    "v_exp_f32 v2, v2\n"
    "v_exp_f32 v3, v3\n"
    "v_exp_f32 v4, v4\n"
    "v_exp_f32 v5, v5\n"
    "v_mfma_f32_32x32x16_bf16 v[16:31], v[92:95], v[84:87], v[112:127]\n"
    "v_exp_f32 v6, v6\n"
    "v_exp_f32 v7, v7\n"
    "v_exp_f32 v8, v8\n"
    "v_exp_f32 v9, v9\n"
    "s_waitcnt lgkmcnt(0)\n"
    "v_smfmac_f32_32x32x32_bf16 v[32:47], v[64:67], v[0:7], v76\n"
    "v_exp_f32 v10, v10\n"
    "v_exp_f32 v11, v11\n"
    "v_exp_f32 v12, v12\n"
    "v_exp_f32 v13, v13\n"
    "v_exp_f32 v14, v14\n"
    "v_exp_f32 v15, v15\n"
    "v_exp_f32 v16, v16\n"
    "v_exp_f32 v17, v17\n"
    "v_smfmac_f32_32x32x32_bf16 v[32:47], v[72:75], v[8:15], v76\n"
    "v_exp_f32 v18, v18\n"
    "v_exp_f32 v19, v19\n"
    "v_exp_f32 v20, v20\n"
    "v_exp_f32 v21, v21\n"
    "v_mfma_f32_32x32x16_bf16 v[0:15], v[88:91], v[80:83], v[96:111]\n"
    "v_exp_f32 v22, v22\n"
    "v_exp_f32 v23, v23\n"
    "v_exp_f32 v24, v24\n"
    "v_exp_f32 v25, v25\n"
    "v_smfmac_f32_32x32x32_bf16 v[48:63], v[64:67], v[16:23], v76\n"
    "ds_read_b128 v[92:95], v250 offset:38912\n"
    "s_mov_b64 exec, s[30:31]\n"
    "ds_read_b128 v[64:67], v250 offset:37888\n"
    "ds_read_b128 v[68:71], v250 offset:38144\n"
    "s_mov_b64 exec, -1\n"
    "v_exp_f32 v26, v26\n"
    "v_exp_f32 v27, v27\n"
    "v_exp_f32 v28, v28\n"
    "v_exp_f32 v29, v29\n"
    "v_exp_f32 v30, v30\n"
    "v_exp_f32 v31, v31\n"
    "buffer_load_dwordx4 v[140:143], v255, s[76:79], s86 offen offset:3072 sc1 nt\n"
    "s_add_u32 s86, s86, 0x1000\n"
    "buffer_load_dwordx4 v[144:147], v255, s[76:79], s86 offen offset:0 sc1 nt\n"
    "buffer_load_dwordx4 v[148:151], v255, s[76:79], s86 offen offset:1024 sc1 nt\n"
    "v_exp_f32 v0, v0\n"
    "v_exp_f32 v1, v1\n"
    "v_smfmac_f32_32x32x32_bf16 v[48:63], v[72:75], v[24:31], v76\n"
    "v_exp_f32 v2, v2\n"
    "v_exp_f32 v3, v3\n"
    "v_exp_f32 v4, v4\n"
    "v_exp_f32 v5, v5\n"
    "v_mfma_f32_32x32x16_bf16 v[16:31], v[88:91], v[84:87], v[112:127]\n"
    "v_exp_f32 v6, v6\n"
    "v_exp_f32 v7, v7\n"
    "v_exp_f32 v8, v8\n"
    "v_exp_f32 v9, v9\n"
    "s_waitcnt lgkmcnt(0)\n"
    "v_smfmac_f32_32x32x32_bf16 v[32:47], v[64:67], v[0:7], v76\n"
    "v_exp_f32 v10, v10\n"
    "v_exp_f32 v11, v11\n"
    "v_exp_f32 v12, v12\n"
    "v_exp_f32 v13, v13\n"
    "v_exp_f32 v14, v14\n"
    "v_exp_f32 v15, v15\n"
    "v_exp_f32 v16, v16\n"
    "v_exp_f32 v17, v17\n"
    "v_smfmac_f32_32x32x32_bf16 v[32:47], v[68:71], v[8:15], v76\n"
    "v_exp_f32 v18, v18\n"
    "v_exp_f32 v19, v19\n"
    "v_exp_f32 v20, v20\n"
    "v_exp_f32 v21, v21\n"
    "v_mfma_f32_32x32x16_bf16 v[0:15], v[92:95], v[80:83], v[96:111]\n"
    "v_exp_f32 v22, v22\n"
    "v_exp_f32 v23, v23\n"
    "v_exp_f32 v24, v24\n"
    "v_exp_f32 v25, v25\n"
    "v_smfmac_f32_32x32x32_bf16 v[48:63], v[64:67], v[16:23], v76\n"
    "ds_read_b128 v[88:91], v250 offset:40960\n"
    "s_mov_b64 exec, s[30:31]\n"
    "ds_read_b128 v[64:67], v250 offset:39936\n"
    "ds_read_b128 v[72:75], v250 offset:40192\n"
    "s_mov_b64 exec, -1\n"
    "v_exp_f32 v26, v26\n"
    "v_exp_f32 v27, v27\n"
    "v_exp_f32 v28, v28\n"
    "v_exp_f32 v29, v29\n"
    "v_exp_f32 v30, v30\n"
    "v_exp_f32 v31, v31\n"
    "buffer_load_dwordx4 v[152:155], v255, s[76:79], s86 offen offset:2048 sc1 nt\n"
    "buffer_load_dwordx4 v[156:159], v255, s[76:79], s86 offen offset:3072 sc1 nt\n"
    "s_add_u32 s86, s86, 0x1000\n"
    "buffer_load_dwordx4 v[160:163], v255, s[76:79], s86 offen offset:0 sc1 nt\n"
    "v_exp_f32 v0, v0\n"
    "v_exp_f32 v1, v1\n"
    "v_smfmac_f32_32x32x32_bf16 v[48:63], v[68:71], v[24:31], v76\n"
    "v_exp_f32 v2, v2\n"
    "v_exp_f32 v3, v3\n"
    "v_exp_f32 v4, v4\n"
    "v_exp_f32 v5, v5\n"
    "v_mfma_f32_32x32x16_bf16 v[16:31], v[92:95], v[84:87], v[112:127]\n"
    "v_exp_f32 v6, v6\n"
    "v_exp_f32 v7, v7\n"
    "v_exp_f32 v8, v8\n"
    "v_exp_f32 v9, v9\n"
    "s_waitcnt lgkmcnt(0)\n"
    "v_smfmac_f32_32x32x32_bf16 v[32:47], v[64:67], v[0:7], v76\n"
    "v_exp_f32 v10, v10\n"
    "v_exp_f32 v11, v11\n"
    "v_exp_f32 v12, v12\n"
    "v_exp_f32 v13, v13\n"
    "v_exp_f32 v14, v14\n"
    "v_exp_f32 v15, v15\n"
    "v_exp_f32 v16, v16\n"
    "v_exp_f32 v17, v17\n"
    "v_smfmac_f32_32x32x32_bf16 v[32:47], v[72:75], v[8:15], v76\n"
    "v_exp_f32 v18, v18\n"
    "v_exp_f32 v19, v19\n"
    "v_exp_f32 v20, v20\n"
    "v_exp_f32 v21, v21\n"
    "v_mfma_f32_32x32x16_bf16 v[0:15], v[88:91], v[80:83], v[96:111]\n"
    "v_exp_f32 v22, v22\n"
    "v_exp_f32 v23, v23\n"
    "v_exp_f32 v24, v24\n"
    "v_exp_f32 v25, v25\n"
    "v_smfmac_f32_32x32x32_bf16 v[48:63], v[64:67], v[16:23], v76\n"
    "ds_read_b128 v[92:95], v250 offset:43008\n"
    "s_mov_b64 exec, s[30:31]\n"
    "ds_read_b128 v[64:67], v250 offset:41984\n"
    "ds_read_b128 v[68:71], v250 offset:42240\n"
    "s_mov_b64 exec, -1\n"
    "v_exp_f32 v26, v26\n"
    "v_exp_f32 v27, v27\n"
    "v_exp_f32 v28, v28\n"
    "v_exp_f32 v29, v29\n"
    "v_exp_f32 v30, v30\n"
    "v_exp_f32 v31, v31\n"
    "buffer_load_dwordx4 v[164:167], v255, s[76:79], s86 offen offset:1024 sc1 nt\n"
    "buffer_load_dwordx4 v[168:171], v255, s[76:79], s86 offen offset:2048 sc1 nt\n"
    "buffer_load_dwordx4 v[172:175], v255, s[76:79], s86 offen offset:3072 sc1 nt\n"
    "s_add_u32 s86, s86, 0x1000\n"
    "v_exp_f32 v0, v0\n"
    "v_exp_f32 v1, v1\n"
    "v_smfmac_f32_32x32x32_bf16 v[48:63], v[72:75], v[24:31], v76\n"
    "v_exp_f32 v2, v2\n"
    "v_exp_f32 v3, v3\n"
    "v_exp_f32 v4, v4\n"
    "v_exp_f32 v5, v5\n"
    "v_mfma_f32_32x32x16_bf16 v[16:31], v[88:91], v[84:87], v[112:127]\n"
    "v_exp_f32 v6, v6\n"
    "v_exp_f32 v7, v7\n"
    "v_exp_f32 v8, v8\n"
    "v_exp_f32 v9, v9\n"
    "s_waitcnt lgkmcnt(0)\n"
    "v_smfmac_f32_32x32x32_bf16 v[32:47], v[64:67], v[0:7], v76\n"
    "v_exp_f32 v10, v10\n"
    "v_exp_f32 v11, v11\n"
    "v_exp_f32 v12, v12\n"
    "v_exp_f32 v13, v13\n"
    "v_exp_f32 v14, v14\n"
    "v_exp_f32 v15, v15\n"
    "v_exp_f32 v16, v16\n"
    "v_exp_f32 v17, v17\n"
    "v_smfmac_f32_32x32x32_bf16 v[32:47], v[68:71], v[8:15], v76\n"
    "v_exp_f32 v18, v18\n"
    "v_exp_f32 v19, v19\n"
    "v_exp_f32 v20, v20\n"
    "v_exp_f32 v21, v21\n"
    "v_mfma_f32_32x32x16_bf16 v[0:15], v[92:95], v[80:83], v[96:111]\n"
    "v_exp_f32 v22, v22\n"
    "v_exp_f32 v23, v23\n"
    "v_exp_f32 v24, v24\n"
    "v_exp_f32 v25, v25\n"
    "v_smfmac_f32_32x32x32_bf16 v[48:63], v[64:67], v[16:23], v76\n"
    "ds_read_b128 v[88:91], v250 offset:45056\n"
    "s_mov_b64 exec, s[30:31]\n"
    "ds_read_b128 v[64:67], v250 offset:44032\n"
    "ds_read_b128 v[72:75], v250 offset:44288\n"
    "s_mov_b64 exec, -1\n"
    "v_exp_f32 v26, v26\n"
    "v_exp_f32 v27, v27\n"
    "v_exp_f32 v28, v28\n"
    "v_exp_f32 v29, v29\n"
    "v_exp_f32 v30, v30\n"
    "v_exp_f32 v31, v31\n"
    "buffer_load_dwordx4 v[176:179], v255, s[76:79], s86 offen offset:0 sc1 nt\n"
    "buffer_load_dwordx4 v[180:183], v255, s[76:79], s86 offen offset:1024 sc1 nt\n"
    "buffer_load_dwordx4 v[184:187], v255, s[76:79], s86 offen offset:2048 sc1 nt\n"
    "v_exp_f32 v0, v0\n"
    "v_exp_f32 v1, v1\n"
    "v_smfmac_f32_32x32x32_bf16 v[48:63], v[68:71], v[24:31], v76\n"
    "v_exp_f32 v2, v2\n"
    "v_exp_f32 v3, v3\n"
    "v_exp_f32 v4, v4\n"
    "v_exp_f32 v5, v5\n"
    "v_mfma_f32_32x32x16_bf16 v[16:31], v[92:95], v[84:87], v[112:127]\n"
    "v_exp_f32 v6, v6\n"
    "v_exp_f32 v7, v7\n"
    "v_exp_f32 v8, v8\n"
    "v_exp_f32 v9, v9\n"
    "s_waitcnt lgkmcnt(0)\n"
    "v_smfmac_f32_32x32x32_bf16 v[32:47], v[64:67], v[0:7], v76\n"
    "v_exp_f32 v10, v10\n"
    "v_exp_f32 v11, v11\n"
    "v_exp_f32 v12, v12\n"
    "v_exp_f32 v13, v13\n"
    "v_exp_f32 v14, v14\n"
    "v_exp_f32 v15, v15\n"
    "v_exp_f32 v16, v16\n"
    "v_exp_f32 v17, v17\n"
    "v_smfmac_f32_32x32x32_bf16 v[32:47], v[72:75], v[8:15], v76\n"
    "v_exp_f32 v18, v18\n"
    "v_exp_f32 v19, v19\n"
    "v_exp_f32 v20, v20\n"
    "v_exp_f32 v21, v21\n"
    "v_mfma_f32_32x32x16_bf16 v[0:15], v[88:91], v[80:83], v[96:111]\n"
    "v_exp_f32 v22, v22\n"
    "v_exp_f32 v23, v23\n"
    "v_exp_f32 v24, v24\n"
    "v_exp_f32 v25, v25\n"
    "v_smfmac_f32_32x32x32_bf16 v[48:63], v[64:67], v[16:23], v76\n"
    "ds_read_b128 v[92:95], v250 offset:47104\n"
    "s_mov_b64 exec, s[30:31]\n"
    "ds_read_b128 v[64:67], v250 offset:46080\n"
    "ds_read_b128 v[68:71], v250 offset:46336\n"
    "s_mov_b64 exec, -1\n"
    "v_exp_f32 v26, v26\n"
    "v_exp_f32 v27, v27\n"
    "v_exp_f32 v28, v28\n"
    "v_exp_f32 v29, v29\n"
    "v_exp_f32 v30, v30\n"
    "v_exp_f32 v31, v31\n"
    "buffer_load_dwordx4 v[188:191], v255, s[76:79], s86 offen offset:3072 sc1 nt\n"
    "s_add_u32 s86, s86, 0x1000\n"
    "buffer_load_dwordx4 v[192:195], v255, s[76:79], s86 offen offset:0 sc1 nt\n"
    "buffer_load_dwordx4 v[196:199], v255, s[76:79], s86 offen offset:1024 sc1 nt\n"
    "v_exp_f32 v0, v0\n"
    "v_exp_f32 v1, v1\n"
    "v_smfmac_f32_32x32x32_bf16 v[48:63], v[72:75], v[24:31], v76\n"
    "v_exp_f32 v2, v2\n"
    "v_exp_f32 v3, v3\n"
    "v_exp_f32 v4, v4\n"
    "v_exp_f32 v5, v5\n"
    "v_mfma_f32_32x32x16_bf16 v[16:31], v[88:91], v[84:87], v[112:127]\n"
    "v_exp_f32 v6, v6\n"
    "v_exp_f32 v7, v7\n"
    "v_exp_f32 v8, v8\n"
    "v_exp_f32 v9, v9\n"
    "s_waitcnt lgkmcnt(0)\n"
    "v_smfmac_f32_32x32x32_bf16 v[32:47], v[64:67], v[0:7], v76\n"
    "v_exp_f32 v10, v10\n"
    "v_exp_f32 v11, v11\n"
    "v_exp_f32 v12, v12\n"
    "v_exp_f32 v13, v13\n"
    "v_exp_f32 v14, v14\n"
    "v_exp_f32 v15, v15\n"
    "v_exp_f32 v16, v16\n"
    "v_exp_f32 v17, v17\n"
    "v_smfmac_f32_32x32x32_bf16 v[32:47], v[68:71], v[8:15], v76\n"
    "v_exp_f32 v18, v18\n"
    "v_exp_f32 v19, v19\n"
    "v_exp_f32 v20, v20\n"
    "v_exp_f32 v21, v21\n"
    "v_mfma_f32_32x32x16_bf16 v[0:15], v[92:95], v[80:83], v[96:111]\n"
    "v_exp_f32 v22, v22\n"
    "v_exp_f32 v23, v23\n"
    "v_exp_f32 v24, v24\n"
    "v_exp_f32 v25, v25\n"
    "v_smfmac_f32_32x32x32_bf16 v[48:63], v[64:67], v[16:23], v76\n"
    "ds_read_b128 v[88:91], v77 offset:0\n"
    "s_mov_b64 exec, s[30:31]\n"
    "ds_read_b128 v[64:67], v250 offset:48128\n"
    "ds_read_b128 v[72:75], v250 offset:48384\n"
    "s_mov_b64 exec, -1\n"
    "v_exp_f32 v26, v26\n"
    "v_exp_f32 v27, v27\n"
    "v_exp_f32 v28, v28\n"
    "v_exp_f32 v29, v29\n"
    "v_exp_f32 v30, v30\n"
    "v_exp_f32 v31, v31\n"
    "buffer_load_dwordx4 v[200:203], v255, s[76:79], s86 offen offset:2048 sc1 nt\n"
    "buffer_load_dwordx4 v[204:207], v255, s[76:79], s86 offen offset:3072 sc1 nt\n"
    "s_add_u32 s86, s86, 0x1000\n"
    "buffer_load_dwordx4 v[208:211], v255, s[76:79], s86 offen offset:0 sc1 nt\n"
    "buffer_load_dwordx4 v[212:215], v255, s[76:79], s86 offen offset:1024 sc1 nt\n"
    "v_exp_f32 v0, v0\n"
    "v_exp_f32 v1, v1\n"
    "v_smfmac_f32_32x32x32_bf16 v[48:63], v[68:71], v[24:31], v76\n"
    "v_exp_f32 v2, v2\n"
    "v_exp_f32 v3, v3\n"
    "v_exp_f32 v4, v4\n"
    "v_exp_f32 v5, v5\n"
    "v_mfma_f32_32x32x16_bf16 v[16:31], v[92:95], v[84:87], v[112:127]\n"
    "v_exp_f32 v6, v6\n"
    "v_exp_f32 v7, v7\n"
    "v_exp_f32 v8, v8\n"
    "v_exp_f32 v9, v9\n"
    "s_waitcnt lgkmcnt(0)\n"
    "v_smfmac_f32_32x32x32_bf16 v[32:47], v[64:67], v[0:7], v76\n"
    "v_exp_f32 v10, v10\n"
    "v_exp_f32 v11, v11\n"
    "v_exp_f32 v12, v12\n"
    "v_exp_f32 v13, v13\n"
    "v_exp_f32 v14, v14\n"
    "v_exp_f32 v15, v15\n"
    "v_exp_f32 v16, v16\n"
    "v_exp_f32 v17, v17\n"
    "v_smfmac_f32_32x32x32_bf16 v[32:47], v[72:75], v[8:15], v76\n"
    "v_exp_f32 v18, v18\n"
    "v_exp_f32 v19, v19\n"
    "v_exp_f32 v20, v20\n"
    "v_exp_f32 v21, v21\n"
    "v_mfma_f32_32x32x16_bf16 v[0:15], v[88:91], v[80:83], v[96:111]\n"
    "v_exp_f32 v22, v22\n"
    "v_exp_f32 v23, v23\n"
    "v_exp_f32 v24, v24\n"
    "v_exp_f32 v25, v25\n"
    "v_smfmac_f32_32x32x32_bf16 v[48:63], v[64:67], v[16:23], v76\n"
    "ds_read_b128 v[92:95], v77 offset:2048\n"
    "s_mov_b64 exec, s[30:31]\n"
    "ds_read_b128 v[64:67], v77 offset:1024\n"
    "ds_read_b128 v[68:71], v77 offset:1280\n"
    "s_mov_b64 exec, -1\n"
    "v_exp_f32 v26, v26\n"
    "v_exp_f32 v27, v27\n"
    "v_exp_f32 v28, v28\n"
    "v_exp_f32 v29, v29\n"
    "v_exp_f32 v30, v30\n"
    "v_exp_f32 v31, v31\n"
    "s_lshl_b32 s21, s6, 7\n"
    "s_lshl_b32 s23, s12, 5\n"
    "s_add_u32 s21, s21, s23\n"
    "s_lshl_b32 s23, s87, 15\n"
    "s_add_u32 s38, s23, s21\n"
    "buffer_load_dwordx2 v[216:217], v240, s[40:43], s38 offen\n"
    "buffer_load_dwordx2 v[218:219], v240, s[40:43], s38 offen offset:16\n"
    "buffer_load_dwordx2 v[220:221], v242, s[72:75], s21 offen\n"
    "buffer_load_dwordx2 v[222:223], v242, s[72:75], s21 offen offset:16\n"
    "s_and_b32 s21, s4, 3\n"
    "s_lshl_b32 s21, s21, 2\n"
    "s_add_u32 s21, s21, 158752\n"
    "s_add_u32 s23, s21, 32\n"
    "s_cmp_eq_u32 s36, 0\n"
    "s_cselect_b32 s21, s23, s21\n"
    "v_mov_b32 v78, s21\n"
    "v_mov_b32 v79, 1\n"
    "ds_write_b32 v78, v79\n"
    "v_exp_f32 v0, v0\n"
    "v_exp_f32 v1, v1\n"
    "v_smfmac_f32_32x32x32_bf16 v[48:63], v[72:75], v[24:31], v76\n"
    "v_exp_f32 v2, v2\n"
    "v_exp_f32 v3, v3\n"
    "v_exp_f32 v4, v4\n"
    "v_exp_f32 v5, v5\n"
    "v_mfma_f32_32x32x16_bf16 v[16:31], v[88:91], v[84:87], v[112:127]\n"
    "v_exp_f32 v6, v6\n"
    "v_exp_f32 v7, v7\n"
    "v_exp_f32 v8, v8\n"
    "v_exp_f32 v9, v9\n"
    "s_waitcnt lgkmcnt(0)\n"
    "v_smfmac_f32_32x32x32_bf16 v[32:47], v[64:67], v[0:7], v76\n"
    "v_exp_f32 v10, v10\n"
    "v_exp_f32 v11, v11\n"
    "v_exp_f32 v12, v12\n"
    "v_exp_f32 v13, v13\n"
    "v_exp_f32 v14, v14\n"
    "v_exp_f32 v15, v15\n"
    "v_exp_f32 v16, v16\n"
    "v_exp_f32 v17, v17\n"
    "v_smfmac_f32_32x32x32_bf16 v[32:47], v[68:71], v[8:15], v76\n"
    "v_exp_f32 v18, v18\n"
    "v_exp_f32 v19, v19\n"
    "v_exp_f32 v20, v20\n"
    "v_exp_f32 v21, v21\n"
    "v_mfma_f32_32x32x16_bf16 v[0:15], v[92:95], v[80:83], v[96:111]\n"
    "v_exp_f32 v22, v22\n"
    "v_exp_f32 v23, v23\n"
    "v_exp_f32 v24, v24\n"
    "v_exp_f32 v25, v25\n"
    "v_smfmac_f32_32x32x32_bf16 v[48:63], v[64:67], v[16:23], v76\n"
    "ds_read_b128 v[88:91], v77 offset:4096\n"
    "s_mov_b64 exec, s[30:31]\n"
    "ds_read_b128 v[64:67], v77 offset:3072\n"
    "ds_read_b128 v[72:75], v77 offset:3328\n"
    "s_mov_b64 exec, -1\n"
    "v_exp_f32 v26, v26\n"
    "v_exp_f32 v27, v27\n"
    "v_exp_f32 v28, v28\n"
    "v_exp_f32 v29, v29\n"
    "v_exp_f32 v30, v30\n"
    "v_exp_f32 v31, v31\n"
    "s_cmp_eq_u32 s36, 1\n"
    "s_cbranch_scc0 k53_%=\n"
    "s_mov_b32 s24, 0\n"
    "sg54_%=:\n"
    "ds_read_b32 v78, v247 offset:32\n"
    "s_add_u32 s24, s24, 1\n"
    "s_waitcnt lgkmcnt(0)\n"
    "v_cmp_ne_u32 s[26:27], 1, v78\n"
    "s_cmp_gt_u32 s24, 0x100000\n"
    "s_cbranch_scc1 so55_%=\n"
    "s_cmp_eq_u64 s[26:27], 0\n"
    "s_cbranch_scc0 sg54_%=\n"
    "so55_%=:\n"
    "s_and_b32 s21, s4, 3\n"
    "s_lshl_b32 s23, s21, 11\n"
    "s_add_u32 s21, s21, 0\n"
    "s_lshl_b32 s21, s21, 14\n"
    "s_lshl_b32 s24, s6, 7\n"
    "s_add_u32 s21, s21, s24\n"
    "s_add_u32 m0, s23, 49152\n"
    "s_nop 0\n"
    "buffer_load_dwordx4 v253, s[68:71], s21 offen lds\n"
    "s_add_u32 m0, s23, 50176\n"
    "s_nop 0\n"
    "buffer_load_dwordx4 v241, s[68:71], s21 offen lds\n"
    "s_and_b32 s21, s4, 3\n"
    "s_lshl_b32 s23, s21, 11\n"
    "s_add_u32 s21, s21, 4\n"
    "s_lshl_b32 s21, s21, 14\n"
    "s_lshl_b32 s24, s6, 7\n"
    "s_add_u32 s21, s21, s24\n"
    "s_add_u32 m0, s23, 57344\n"
    "s_nop 0\n"
    "buffer_load_dwordx4 v253, s[68:71], s21 offen lds\n"
    "s_add_u32 m0, s23, 58368\n"
    "s_nop 0\n"
    "buffer_load_dwordx4 v241, s[68:71], s21 offen lds\n"
    "k53_%=:\n"
    "v_exp_f32 v0, v0\n"
    "v_exp_f32 v1, v1\n"
    "v_smfmac_f32_32x32x32_bf16 v[48:63], v[68:71], v[24:31], v76\n"
    "v_exp_f32 v2, v2\n"
    "v_exp_f32 v3, v3\n"
    "v_exp_f32 v4, v4\n"
    "v_exp_f32 v5, v5\n"
    "v_mfma_f32_32x32x16_bf16 v[16:31], v[92:95], v[84:87], v[112:127]\n"
    "v_exp_f32 v6, v6\n"
    "v_exp_f32 v7, v7\n"
    "v_exp_f32 v8, v8\n"
    "v_exp_f32 v9, v9\n"
    "s_waitcnt lgkmcnt(0)\n"
    "v_smfmac_f32_32x32x32_bf16 v[32:47], v[64:67], v[0:7], v76\n"
    "v_exp_f32 v10, v10\n"
    "v_exp_f32 v11, v11\n"
    "v_exp_f32 v12, v12\n"
    "v_exp_f32 v13, v13\n"
    "v_exp_f32 v14, v14\n"
    "v_exp_f32 v15, v15\n"
    "v_exp_f32 v16, v16\n"
    "v_exp_f32 v17, v17\n"
    "v_smfmac_f32_32x32x32_bf16 v[32:47], v[72:75], v[8:15], v76\n"
    "v_exp_f32 v18, v18\n"
    "v_exp_f32 v19, v19\n"
    "v_exp_f32 v20, v20\n"
    "v_exp_f32 v21, v21\n"
    "v_mfma_f32_32x32x16_bf16 v[0:15], v[88:91], v[80:83], v[96:111]\n"
    "v_exp_f32 v22, v22\n"
    "v_exp_f32 v23, v23\n"
    "v_exp_f32 v24, v24\n"
    "v_exp_f32 v25, v25\n"
    "v_smfmac_f32_32x32x32_bf16 v[48:63], v[64:67], v[16:23], v76\n"
    "ds_read_b128 v[92:95], v77 offset:6144\n"
    "s_mov_b64 exec, s[30:31]\n"
    "ds_read_b128 v[64:67], v77 offset:5120\n"
    "ds_read_b128 v[68:71], v77 offset:5376\n"
    "s_mov_b64 exec, -1\n"
    "v_exp_f32 v26, v26\n"
    "v_exp_f32 v27, v27\n"
    "v_exp_f32 v28, v28\n"
    "v_exp_f32 v29, v29\n"
    "v_exp_f32 v30, v30\n"
    "v_exp_f32 v31, v31\n"
    "s_cmp_eq_u32 s36, 1\n"
    "s_cbranch_scc0 k56_%=\n"
    "s_and_b32 s21, s4, 3\n"
    "s_lshl_b32 s23, s21, 11\n"
    "s_add_u32 s21, s21, 8\n"
    "s_lshl_b32 s21, s21, 14\n"
    "s_lshl_b32 s24, s6, 7\n"
    "s_add_u32 s21, s21, s24\n"
    "s_add_u32 m0, s23, 81920\n"
    "s_nop 0\n"
    "buffer_load_dwordx4 v253, s[68:71], s21 offen lds\n"
    "s_add_u32 m0, s23, 82944\n"
    "s_nop 0\n"
    "buffer_load_dwordx4 v241, s[68:71], s21 offen lds\n"
    "s_and_b32 s21, s4, 3\n"
    "s_lshl_b32 s23, s21, 11\n"
    "s_add_u32 s21, s21, 12\n"
    "s_lshl_b32 s21, s21, 14\n"
    "s_lshl_b32 s24, s6, 7\n"
    "s_add_u32 s21, s21, s24\n"
    "s_add_u32 m0, s23, 90112\n"
    "s_nop 0\n"
    "buffer_load_dwordx4 v253, s[68:71], s21 offen lds\n"
    "s_add_u32 m0, s23, 91136\n"
    "s_nop 0\n"
    "buffer_load_dwordx4 v241, s[68:71], s21 offen lds\n"
    "k56_%=:\n"
    "v_exp_f32 v0, v0\n"
    "v_exp_f32 v1, v1\n"
    "v_smfmac_f32_32x32x32_bf16 v[48:63], v[72:75], v[24:31], v76\n"
    "v_exp_f32 v2, v2\n"
    "v_exp_f32 v3, v3\n"
    "v_exp_f32 v4, v4\n"
    "v_exp_f32 v5, v5\n"
    "v_mfma_f32_32x32x16_bf16 v[16:31], v[88:91], v[84:87], v[112:127]\n"
    "v_exp_f32 v6, v6\n"
    "v_exp_f32 v7, v7\n"
    "v_exp_f32 v8, v8\n"
    "v_exp_f32 v9, v9\n"
    "s_waitcnt lgkmcnt(0)\n"
    "v_smfmac_f32_32x32x32_bf16 v[32:47], v[64:67], v[0:7], v76\n"
    "v_exp_f32 v10, v10\n"
    "v_exp_f32 v11, v11\n"
    "v_exp_f32 v12, v12\n"
    "v_exp_f32 v13, v13\n"
    "v_exp_f32 v14, v14\n"
    "v_exp_f32 v15, v15\n"
    "s_setprio 0\n"
    "v_exp_f32 v16, v16\n"
    "v_exp_f32 v17, v17\n"
    "v_smfmac_f32_32x32x32_bf16 v[32:47], v[68:71], v[8:15], v76\n"
    "v_exp_f32 v18, v18\n"
    "v_exp_f32 v19, v19\n"
    "v_exp_f32 v20, v20\n"
    "v_exp_f32 v21, v21\n"
    "v_mfma_f32_32x32x16_bf16 v[0:15], v[92:95], v[80:83], v[96:111]\n"
    "v_exp_f32 v22, v22\n"
    "v_exp_f32 v23, v23\n"
    "v_exp_f32 v24, v24\n"
    "v_exp_f32 v25, v25\n"
    "v_smfmac_f32_32x32x32_bf16 v[48:63], v[64:67], v[16:23], v76\n"
    "ds_read_b128 v[88:91], v77 offset:8192\n"
    "s_mov_b64 exec, s[30:31]\n"
    "ds_read_b128 v[64:67], v77 offset:7168\n"
    "ds_read_b128 v[72:75], v77 offset:7424\n"
    "s_mov_b64 exec, -1\n"
    "v_exp_f32 v26, v26\n"
    "v_exp_f32 v27, v27\n"
    "v_exp_f32 v28, v28\n"
    "v_exp_f32 v29, v29\n"
    "v_exp_f32 v30, v30\n"
    "v_exp_f32 v31, v31\n"
    "s_cmp_eq_u32 s36, 1\n"
    "s_cbranch_scc0 k57_%=\n"
    "s_and_b32 s21, s4, 3\n"
    "s_lshl_b32 s23, s21, 11\n"
    "s_add_u32 s21, s21, 16\n"
    "s_lshl_b32 s21, s21, 14\n"
    "s_lshl_b32 s24, s6, 7\n"
    "s_add_u32 s21, s21, s24\n"
    "s_add_u32 m0, s23, 114688\n"
    "s_nop 0\n"
    "buffer_load_dwordx4 v253, s[68:71], s21 offen lds\n"
    "s_add_u32 m0, s23, 115712\n"
    "s_nop 0\n"
    "buffer_load_dwordx4 v241, s[68:71], s21 offen lds\n"
    "s_and_b32 s21, s4, 3\n"
    "s_lshl_b32 s23, s21, 11\n"
    "s_add_u32 s21, s21, 20\n"
    "s_lshl_b32 s21, s21, 14\n"
    "s_lshl_b32 s24, s6, 7\n"
    "s_add_u32 s21, s21, s24\n"
    "s_add_u32 m0, s23, 122880\n"
    "s_nop 0\n"
    "buffer_load_dwordx4 v253, s[68:71], s21 offen lds\n"
    "s_add_u32 m0, s23, 123904\n"
    "s_nop 0\n"
    "buffer_load_dwordx4 v241, s[68:71], s21 offen lds\n"
    "k57_%=:\n"
    "v_exp_f32 v0, v0\n"
    "v_exp_f32 v1, v1\n"
    "v_smfmac_f32_32x32x32_bf16 v[48:63], v[68:71], v[24:31], v76\n"
    "v_exp_f32 v2, v2\n"
    "v_exp_f32 v3, v3\n"
    "v_exp_f32 v4, v4\n"
    "v_exp_f32 v5, v5\n"
    "v_mfma_f32_32x32x16_bf16 v[16:31], v[92:95], v[84:87], v[112:127]\n"
    "v_exp_f32 v6, v6\n"
    "v_exp_f32 v7, v7\n"
    "v_exp_f32 v8, v8\n"
    "v_exp_f32 v9, v9\n"
    "s_waitcnt lgkmcnt(0)\n"
    "v_smfmac_f32_32x32x32_bf16 v[32:47], v[64:67], v[0:7], v76\n"
    "v_exp_f32 v10, v10\n"
    "v_exp_f32 v11, v11\n"
    "v_exp_f32 v12, v12\n"
    "v_exp_f32 v13, v13\n"
    "v_exp_f32 v14, v14\n"
    "v_exp_f32 v15, v15\n"
    "v_exp_f32 v16, v16\n"
    "v_exp_f32 v17, v17\n"
    "v_smfmac_f32_32x32x32_bf16 v[32:47], v[72:75], v[8:15], v76\n"
    "v_exp_f32 v18, v18\n"
    "v_exp_f32 v19, v19\n"
    "v_exp_f32 v20, v20\n"
    "v_exp_f32 v21, v21\n"
    "v_mfma_f32_32x32x16_bf16 v[0:15], v[88:91], v[80:83], v[96:111]\n"
    "v_exp_f32 v22, v22\n"
    "v_exp_f32 v23, v23\n"
    "v_exp_f32 v24, v24\n"
    "v_exp_f32 v25, v25\n"
    "v_smfmac_f32_32x32x32_bf16 v[48:63], v[64:67], v[16:23], v76\n"
    "ds_read_b128 v[92:95], v77 offset:10240\n"
    "s_mov_b64 exec, s[30:31]\n"
    "ds_read_b128 v[64:67], v77 offset:9216\n"
    "ds_read_b128 v[68:71], v77 offset:9472\n"
    "s_mov_b64 exec, -1\n"
    "v_exp_f32 v26, v26\n"
    "v_exp_f32 v27, v27\n"
    "v_exp_f32 v28, v28\n"
    "v_exp_f32 v29, v29\n"
    "v_exp_f32 v30, v30\n"
    "v_exp_f32 v31, v31\n"
    "s_cmp_eq_u32 s2, 0\n"
    "s_cselect_b32 s21, s4, 0\n"
    "s_cmp_eq_u32 s21, 4\n"
    "s_cbranch_scc0 end58_%=\n"
    "v_lshlrev_b32 v251, 6, v254\n"
    "s_movk_i32 s23, 16384\n"
    "ep59_%=:\n"
    "s_mov_b32 s24, 0\n"
    "sp61_%=:\n"
    "buffer_load_dwordx2 v[78:79], v251, s[76:79], s23 offen sc1\n"
    "s_add_u32 s24, s24, 1\n"
    "s_waitcnt vmcnt(0)\n"
    "v_cmp_ne_u64 s[26:27], v[78:79], s[8:9]\n"
    "s_cmp_eq_u64 s[26:27], 0\n"
    "s_cbranch_scc1 ok60_%=\n"
    "s_sleep 2\n"
    "s_cmp_gt_u32 s24, 0x40000\n"
    "s_cbranch_scc0 sp61_%=\n"
    "ok60_%=:\n"
    "s_add_u32 s23, s23, 0x1000\n"
    "s_cmp_lt_u32 s23, 32768\n"
    "s_cbranch_scc1 ep59_%=\n"
    "s_mov_b32 s23, 0\n"
    "s_mov_b64 exec, 1\n"
    "v_mov_b32 v78, s8\n"
    "v_mov_b32 v79, s9\n"
    "buffer_store_dwordx2 v[78:79], off, s[76:79], s23 sc1\n"
    "s_mov_b64 exec, -1\n"
    "end58_%=:\n"
    "v_exp_f32 v0, v0\n"
    "v_exp_f32 v1, v1\n"
    "v_smfmac_f32_32x32x32_bf16 v[48:63], v[72:75], v[24:31], v76\n"
    "v_exp_f32 v2, v2\n"
    "v_exp_f32 v3, v3\n"
    "v_exp_f32 v4, v4\n"
    "v_exp_f32 v5, v5\n"
    "v_mfma_f32_32x32x16_bf16 v[16:31], v[88:91], v[84:87], v[112:127]\n"
    "v_exp_f32 v6, v6\n"
    "v_exp_f32 v7, v7\n"
    "v_exp_f32 v8, v8\n"
    "v_exp_f32 v9, v9\n"
    "s_waitcnt lgkmcnt(0)\n"
    "v_smfmac_f32_32x32x32_bf16 v[32:47], v[64:67], v[0:7], v76\n"
    "v_exp_f32 v10, v10\n"
    "v_exp_f32 v11, v11\n"
    "v_exp_f32 v12, v12\n"
    "v_exp_f32 v13, v13\n"
    "v_exp_f32 v14, v14\n"
    "v_exp_f32 v15, v15\n"
    "v_exp_f32 v16, v16\n"
    "v_exp_f32 v17, v17\n"
    "v_smfmac_f32_32x32x32_bf16 v[32:47], v[68:71], v[8:15], v76\n"
    "v_exp_f32 v18, v18\n"
    "v_exp_f32 v19, v19\n"
    "v_exp_f32 v20, v20\n"
    "v_exp_f32 v21, v21\n"
    "v_mfma_f32_32x32x16_bf16 v[0:15], v[92:95], v[80:83], v[96:111]\n"
    "v_exp_f32 v22, v22\n"
    "v_exp_f32 v23, v23\n"
    "v_exp_f32 v24, v24\n"
    "v_exp_f32 v25, v25\n"
    "v_smfmac_f32_32x32x32_bf16 v[48:63], v[64:67], v[16:23], v76\n"
    "ds_read_b128 v[88:91], v77 offset:12288\n"
    "s_mov_b64 exec, s[30:31]\n"
    "ds_read_b128 v[64:67], v77 offset:11264\n"
    "ds_read_b128 v[72:75], v77 offset:11520\n"
    "s_mov_b64 exec, -1\n"
    "v_exp_f32 v26, v26\n"
    "v_exp_f32 v27, v27\n"
    "v_exp_f32 v28, v28\n"
    "v_exp_f32 v29, v29\n"
    "v_exp_f32 v30, v30\n"
    "v_exp_f32 v31, v31\n"
    "s_cmp_eq_u32 s36, 1\n"
    "s_cbranch_scc0 k63_%=\n"
    "s_mov_b32 s24, 0\n"
    "sg64_%=:\n"
    "ds_read_b32 v78, v247 offset:48\n"
    "s_add_u32 s24, s24, 1\n"
    "s_waitcnt lgkmcnt(0)\n"
    "v_cmp_ne_u32 s[26:27], 1, v78\n"
    "s_cmp_gt_u32 s24, 0x100000\n"
    "s_cbranch_scc1 so65_%=\n"
    "s_cmp_eq_u64 s[26:27], 0\n"
    "s_cbranch_scc0 sg64_%=\n"
    "so65_%=:\n"
    "s_and_b32 s21, s4, 3\n"
    "s_lshl_b32 s23, s21, 11\n"
    "s_add_u32 s21, s21, 24\n"
    "s_lshl_b32 s21, s21, 14\n"
    "s_lshl_b32 s24, s6, 7\n"
    "s_add_u32 s21, s21, s24\n"
    "s_add_u32 m0, s23, 32768\n"
    "s_nop 0\n"
    "buffer_load_dwordx4 v253, s[68:71], s21 offen lds\n"
    "s_add_u32 m0, s23, 33792\n"
    "s_nop 0\n"
    "buffer_load_dwordx4 v241, s[68:71], s21 offen lds\n"
    "s_and_b32 s21, s4, 3\n"
    "s_lshl_b32 s23, s21, 11\n"
    "s_add_u32 s21, s21, 28\n"
    "s_lshl_b32 s21, s21, 14\n"
    "s_lshl_b32 s24, s6, 7\n"
    "s_add_u32 s21, s21, s24\n"
    "s_add_u32 m0, s23, 40960\n"
    "s_nop 0\n"
    "buffer_load_dwordx4 v253, s[68:71], s21 offen lds\n"
    "s_add_u32 m0, s23, 41984\n"
    "s_nop 0\n"
    "buffer_load_dwordx4 v241, s[68:71], s21 offen lds\n"
    "k63_%=:\n"
    "v_exp_f32 v0, v0\n"
    "v_exp_f32 v1, v1\n"
    "v_smfmac_f32_32x32x32_bf16 v[48:63], v[68:71], v[24:31], v76\n"
    "v_exp_f32 v2, v2\n"
    "v_exp_f32 v3, v3\n"
    "v_exp_f32 v4, v4\n"
    "v_exp_f32 v5, v5\n"
    "v_mfma_f32_32x32x16_bf16 v[16:31], v[92:95], v[84:87], v[112:127]\n"
    "v_exp_f32 v6, v6\n"
    "v_exp_f32 v7, v7\n"
    "v_exp_f32 v8, v8\n"
    "v_exp_f32 v9, v9\n"
    "s_waitcnt lgkmcnt(0)\n"
    "v_smfmac_f32_32x32x32_bf16 v[32:47], v[64:67], v[0:7], v76\n"
    "v_exp_f32 v10, v10\n"
    "v_exp_f32 v11, v11\n"
    "v_exp_f32 v12, v12\n"
    "v_exp_f32 v13, v13\n"
    "v_exp_f32 v14, v14\n"
    "v_exp_f32 v15, v15\n"
    "v_exp_f32 v16, v16\n"
    "v_exp_f32 v17, v17\n"
    "v_smfmac_f32_32x32x32_bf16 v[32:47], v[72:75], v[8:15], v76\n"
    "v_exp_f32 v18, v18\n"
    "v_exp_f32 v19, v19\n"
    "v_exp_f32 v20, v20\n"
    "v_exp_f32 v21, v21\n"
    "v_mfma_f32_32x32x16_bf16 v[0:15], v[88:91], v[80:83], v[96:111]\n"
    "v_exp_f32 v22, v22\n"
    "v_exp_f32 v23, v23\n"
    "v_exp_f32 v24, v24\n"
    "v_exp_f32 v25, v25\n"
    "v_smfmac_f32_32x32x32_bf16 v[48:63], v[64:67], v[16:23], v76\n"
    "ds_read_b128 v[92:95], v77 offset:14336\n"
    "s_mov_b64 exec, s[30:31]\n"
    "ds_read_b128 v[64:67], v77 offset:13312\n"
    "ds_read_b128 v[68:71], v77 offset:13568\n"
    "s_mov_b64 exec, -1\n"
    "v_exp_f32 v26, v26\n"
    "v_exp_f32 v27, v27\n"
    "v_exp_f32 v28, v28\n"
    "v_exp_f32 v29, v29\n"
    "v_exp_f32 v30, v30\n"
    "v_exp_f32 v31, v31\n"
    "s_cmp_eq_u32 s36, 1\n"
    "s_cbranch_scc0 k66_%=\n"
    "s_and_b32 s21, s4, 3\n"
    "s_lshl_b32 s23, s21, 11\n"
    "s_add_u32 s21, s21, 32\n"
    "s_lshl_b32 s21, s21, 14\n"
    "s_lshl_b32 s24, s6, 7\n"
    "s_add_u32 s21, s21, s24\n"
    "s_add_u32 m0, s23, 65536\n"
    "s_nop 0\n"
    "buffer_load_dwordx4 v253, s[68:71], s21 offen lds\n"
    "s_add_u32 m0, s23, 66560\n"
    "s_nop 0\n"
    "buffer_load_dwordx4 v241, s[68:71], s21 offen lds\n"
    "s_and_b32 s21, s4, 3\n"
    "s_lshl_b32 s23, s21, 11\n"
    "s_add_u32 s21, s21, 36\n"
    "s_lshl_b32 s21, s21, 14\n"
    "s_lshl_b32 s24, s6, 7\n"
    "s_add_u32 s21, s21, s24\n"
    "s_add_u32 m0, s23, 73728\n"
    "s_nop 0\n"
    "buffer_load_dwordx4 v253, s[68:71], s21 offen lds\n"
    "s_add_u32 m0, s23, 74752\n"
    "s_nop 0\n"
    "buffer_load_dwordx4 v241, s[68:71], s21 offen lds\n"
    "k66_%=:\n"
    "v_exp_f32 v0, v0\n"
    "v_exp_f32 v1, v1\n"
    "v_smfmac_f32_32x32x32_bf16 v[48:63], v[72:75], v[24:31], v76\n"
    "v_exp_f32 v2, v2\n"
    "v_exp_f32 v3, v3\n"
    "v_exp_f32 v4, v4\n"
    "v_exp_f32 v5, v5\n"
    "v_mfma_f32_32x32x16_bf16 v[16:31], v[88:91], v[84:87], v[112:127]\n"
    "v_exp_f32 v6, v6\n"
    "v_exp_f32 v7, v7\n"
    "v_exp_f32 v8, v8\n"
    "v_exp_f32 v9, v9\n"
    "s_waitcnt lgkmcnt(0)\n"
    "v_smfmac_f32_32x32x32_bf16 v[32:47], v[64:67], v[0:7], v76\n"
    "v_exp_f32 v10, v10\n"
    "v_exp_f32 v11, v11\n"
    "v_exp_f32 v12, v12\n"
    "v_exp_f32 v13, v13\n"
    "v_exp_f32 v14, v14\n"
    "v_exp_f32 v15, v15\n"
    "v_exp_f32 v16, v16\n"
    "v_exp_f32 v17, v17\n"
    "v_smfmac_f32_32x32x32_bf16 v[32:47], v[68:71], v[8:15], v76\n"
    "v_exp_f32 v18, v18\n"
    "v_exp_f32 v19, v19\n"
    "v_exp_f32 v20, v20\n"
    "v_exp_f32 v21, v21\n"
    "v_mfma_f32_32x32x16_bf16 v[0:15], v[92:95], v[80:83], v[96:111]\n"
    "v_exp_f32 v22, v22\n"
    "v_exp_f32 v23, v23\n"
    "v_exp_f32 v24, v24\n"
    "v_exp_f32 v25, v25\n"
    "v_smfmac_f32_32x32x32_bf16 v[48:63], v[64:67], v[16:23], v76\n"
    "s_mov_b64 exec, s[30:31]\n"
    "ds_read_b128 v[64:67], v77 offset:15360\n"
    "ds_read_b128 v[72:75], v77 offset:15616\n"
    "s_mov_b64 exec, -1\n"
    "v_exp_f32 v26, v26\n"
    "v_exp_f32 v27, v27\n"
    "v_exp_f32 v28, v28\n"
    "v_exp_f32 v29, v29\n"
    "v_exp_f32 v30, v30\n"
    "v_exp_f32 v31, v31\n"
    "s_cmp_eq_u32 s36, 1\n"
    "s_cbranch_scc0 k67_%=\n"
    "s_mov_b32 s24, 0\n"
    "sg68_%=:\n"
    "ds_read_b32 v78, v247 offset:64\n"
    "s_add_u32 s24, s24, 1\n"
    "s_waitcnt lgkmcnt(0)\n"
    "v_cmp_ne_u32 s[26:27], 1, v78\n"
    "s_cmp_gt_u32 s24, 0x100000\n"
    "s_cbranch_scc1 so69_%=\n"
    "s_cmp_eq_u64 s[26:27], 0\n"
    "s_cbranch_scc0 sg68_%=\n"
    "so69_%=:\n"
    "s_and_b32 s21, s4, 3\n"
    "s_lshl_b32 s23, s21, 11\n"
    "s_add_u32 s21, s21, 40\n"
    "s_lshl_b32 s21, s21, 14\n"
    "s_lshl_b32 s24, s6, 7\n"
    "s_add_u32 s21, s21, s24\n"
    "s_add_u32 m0, s23, 98304\n"
    "s_nop 0\n"
    "buffer_load_dwordx4 v253, s[68:71], s21 offen lds\n"
    "s_add_u32 m0, s23, 99328\n"
    "s_nop 0\n"
    "buffer_load_dwordx4 v241, s[68:71], s21 offen lds\n"
    "s_and_b32 s21, s4, 3\n"
    "s_lshl_b32 s23, s21, 11\n"
    "s_add_u32 s21, s21, 44\n"
    "s_lshl_b32 s21, s21, 14\n"
    "s_lshl_b32 s24, s6, 7\n"
    "s_add_u32 s21, s21, s24\n"
    "s_add_u32 m0, s23, 106496\n"
    "s_nop 0\n"
    "buffer_load_dwordx4 v253, s[68:71], s21 offen lds\n"
    "s_add_u32 m0, s23, 107520\n"
    "s_nop 0\n"
    "buffer_load_dwordx4 v241, s[68:71], s21 offen lds\n"
    "k67_%=:\n"
    "v_exp_f32 v0, v0\n"
    "v_exp_f32 v1, v1\n"
    "v_smfmac_f32_32x32x32_bf16 v[48:63], v[68:71], v[24:31], v76\n"
    "v_exp_f32 v2, v2\n"
    "v_exp_f32 v3, v3\n"
    "v_exp_f32 v4, v4\n"
    "v_exp_f32 v5, v5\n"
    "v_mfma_f32_32x32x16_bf16 v[16:31], v[92:95], v[84:87], v[112:127]\n"
    "v_exp_f32 v6, v6\n"
    "v_exp_f32 v7, v7\n"
    "v_exp_f32 v8, v8\n"
    "v_exp_f32 v9, v9\n"
    "s_waitcnt lgkmcnt(0)\n"
    "v_smfmac_f32_32x32x32_bf16 v[32:47], v[64:67], v[0:7], v76\n"
    "v_exp_f32 v10, v10\n"
    "v_exp_f32 v11, v11\n"
    "v_exp_f32 v12, v12\n"
    "v_exp_f32 v13, v13\n"
    "v_exp_f32 v14, v14\n"
    "v_exp_f32 v15, v15\n"
    "v_exp_f32 v16, v16\n"
    "v_exp_f32 v17, v17\n"
    "v_smfmac_f32_32x32x32_bf16 v[32:47], v[72:75], v[8:15], v76\n"
    "v_exp_f32 v18, v18\n"
    "v_exp_f32 v19, v19\n"
    "v_exp_f32 v20, v20\n"
    "v_exp_f32 v21, v21\n"
    "v_exp_f32 v22, v22\n"
    "v_exp_f32 v23, v23\n"
    "v_exp_f32 v24, v24\n"
    "v_exp_f32 v25, v25\n"
    "v_smfmac_f32_32x32x32_bf16 v[48:63], v[64:67], v[16:23], v76\n"
    "v_exp_f32 v26, v26\n"
    "v_exp_f32 v27, v27\n"
    "v_exp_f32 v28, v28\n"
    "v_exp_f32 v29, v29\n"
    "v_exp_f32 v30, v30\n"
    "v_exp_f32 v31, v31\n"
    "s_lshl_b32 s22, s10, 1\n"
    "s_add_u32 s17, s4, 48\n"
    "s_lshl_b32 s17, s17, 14\n"
    "v_smfmac_f32_32x32x32_bf16 v[48:63], v[72:75], v[24:31], v76\n"
    "s_lshl_b32 s21, s6, 7\n"
    "s_add_u32 s17, s17, s21\n"
    "s_and_b32 s18, s4, 3\n"
    "s_lshl3_add_u32 s18, s18, s36\n"
    "s_lshl_b32 s18, s18, 14\n"
    "s_add_u32 s18, s18, s21\n"
    "s_lshl_b32 s21, s12, 13\n"
    "s_add_u32 s19, s19, s21\n"
    "s_add_u32 s19, s19, 90112\n"
    "s_add_u32 s37, s19, 0x1000\n"
    "s_nop 1\n"
    "ds_write_b128 v243, v[48:51]\n"
    "ds_write_b128 v243, v[52:55] offset:1024\n"
    "ds_write_b32 v244, v56 offset:2048\n"
    "s_waitcnt vmcnt(0) lgkmcnt(0)\n"
    "s_barrier\n"
    "ds_read_b128 v[48:51], v245\n"
    "ds_read_b128 v[52:55], v245 offset:1024\n"
    "ds_read_b32 v56, v246 offset:2048\n"
    "ds_read_b64_tr_b16 v[96:97], v238 offset:0\n"
    "ds_read_b64_tr_b16 v[98:99], v238 offset:1024\n"
    "ds_read_b64_tr_b16 v[100:101], v239 offset:0\n"
    "ds_read_b64_tr_b16 v[102:103], v239 offset:1024\n"
    "ds_read_b64_tr_b16 v[104:105], v238 offset:8192\n"
    "ds_read_b64_tr_b16 v[106:107], v238 offset:9216\n"
    "ds_read_b64_tr_b16 v[108:109], v239 offset:8192\n"
    "ds_read_b64_tr_b16 v[110:111], v239 offset:9216\n"
    "s_add_u32 m0, s22, 0\n"
    "s_nop 0\n"
    "buffer_load_dwordx4 v253, s[68:71], s17 offen lds\n"
    "s_add_u32 m0, s22, 1024\n"
    "s_nop 0\n"
    "buffer_load_dwordx4 v241, s[68:71], s17 offen lds\n"
    "s_add_u32 s17, s17, 131072\n"
    "s_add_u32 m0, s22, 16384\n"
    "s_nop 0\n"
    "buffer_load_dwordx4 v253, s[68:71], s17 offen lds\n"
    "s_add_u32 m0, s22, 17408\n"
    "s_nop 0\n"
    "buffer_load_dwordx4 v241, s[68:71], s17 offen lds\n"
    "s_add_u32 s17, s17, 131072\n"
    "s_waitcnt lgkmcnt(8)\n"
    "v_pk_add_f32 v[32:33], v[32:33], v[48:49] op_sel:[0,0] op_sel_hi:[1,1]\n"
    "v_pk_add_f32 v[34:35], v[34:35], v[50:51] op_sel:[0,0] op_sel_hi:[1,1]\n"
    "v_pk_add_f32 v[36:37], v[36:37], v[52:53] op_sel:[0,0] op_sel_hi:[1,1]\n"
    "v_pk_add_f32 v[38:39], v[38:39], v[54:55] op_sel:[0,0] op_sel_hi:[1,1]\n"
    "v_add_f32 v40, v40, v56\n"
    "v_pk_add_f32 v[48:49], v[32:33], v[34:35] op_sel:[0,0] op_sel_hi:[1,1]\n"
    "v_pk_add_f32 v[50:51], v[36:37], v[38:39] op_sel:[0,0] op_sel_hi:[1,1]\n"
    "v_pk_add_f32 v[48:49], v[48:49], v[50:51] op_sel:[0,0] op_sel_hi:[1,1]\n"
    "v_add_f32 v78, v48, v49\n"
    "v_mov_b32 v79, 0x7f800000\n"
    "v_cmp_le_f32 vcc, 0x0d800000, v40\n"
    "s_mov_b64 s[28:29], vcc\n"
    "v_cmp_ge_f32 vcc, 0x7b800000, v40\n"
    "v_cmp_lt_f32 s[26:27], |v78|, v79\n"
    "s_and_b64 s[28:29], s[28:29], vcc\n"
    "s_and_b64 s[28:29], s[28:29], s[26:27]\n"
    "s_cmp_eq_u64 s[28:29], -1\n"
    "s_cbranch_scc1 good70_%=\n"
    "s_mov_b32 s23, 131072\n"
    "v_mov_b32 v78, 0xff800000\n"
    "f171_%=:\n"
    "buffer_load_dwordx4 v[88:91], v255, s[76:79], s23 offen sc1\n"
    "s_add_u32 s23, s23, 0x800\n"
    "s_waitcnt vmcnt(0)\n"
    "v_mfma_f32_32x32x16_bf16 v[0:15], v[88:91], v[80:83], 0\n"
    "s_nop 11\n"
    "v_max3_f32 v79, v0, v1, v2\n"
    "v_max3_f32 v79, v79, v3, v4\n"
    "v_max3_f32 v79, v79, v5, v6\n"
    "v_max3_f32 v79, v79, v7, v8\n"
    "v_max3_f32 v79, v79, v9, v10\n"
    "v_max3_f32 v79, v79, v11, v12\n"
    "v_max3_f32 v79, v79, v13, v14\n"
    "v_max_f32 v79, v79, v15\n"
    "v_max_f32 v78, v78, v79\n"
    "s_cmp_lt_u32 s23, 262144\n"
    "s_cbranch_scc1 f171_%=\n"
    "v_mov_b32 v79, v78\n"
    "s_nop 1\n"
    "v_permlane32_swap_b32 v78, v79\n"
    "v_max_f32 v78, v78, v79\n"
    "v_sub_f32 v112, 0, v78\n"
    "v_mov_b32 v32, 0\n"
    "v_sub_f32 v113, 0, v78\n"
    "v_mov_b32 v33, 0\n"
    "v_sub_f32 v114, 0, v78\n"
    "v_mov_b32 v34, 0\n"
    "v_sub_f32 v115, 0, v78\n"
    "v_mov_b32 v35, 0\n"
    "v_sub_f32 v116, 0, v78\n"
    "v_mov_b32 v36, 0\n"
    "v_sub_f32 v117, 0, v78\n"
    "v_mov_b32 v37, 0\n"
    "v_sub_f32 v118, 0, v78\n"
    "v_mov_b32 v38, 0\n"
    "v_sub_f32 v119, 0, v78\n"
    "v_mov_b32 v39, 0\n"
    "v_sub_f32 v120, 0, v78\n"
    "v_mov_b32 v40, 0\n"
    "v_sub_f32 v121, 0, v78\n"
    "v_mov_b32 v41, 0\n"
    "v_sub_f32 v122, 0, v78\n"
    "v_mov_b32 v42, 0\n"
    "v_sub_f32 v123, 0, v78\n"
    "v_mov_b32 v43, 0\n"
    "v_sub_f32 v124, 0, v78\n"
    "v_mov_b32 v44, 0\n"
    "v_sub_f32 v125, 0, v78\n"
    "v_mov_b32 v45, 0\n"
    "v_sub_f32 v126, 0, v78\n"
    "v_mov_b32 v46, 0\n"
    "v_sub_f32 v127, 0, v78\n"
    "v_mov_b32 v47, 0\n"
    "s_mov_b32 s23, 131072\n"
    "f272_%=:\n"
    "buffer_load_dwordx4 v[88:91], v255, s[76:79], s23 offen sc1\n"
    "s_mov_b64 exec, s[30:31]\n"
    "buffer_load_dwordx4 v[64:67], v255, s[76:79], s23 offen offset:1024 sc1\n"
    "buffer_load_dwordx4 v[68:71], v255, s[76:79], s23 offen offset:1280 sc1\n"
    "s_mov_b64 exec, -1\n"
    "s_add_u32 s23, s23, 0x800\n"
    "s_waitcnt vmcnt(0)\n"
    "v_mfma_f32_32x32x16_bf16 v[0:15], v[88:91], v[80:83], v[112:127]\n"
    "s_nop 11\n"
    "v_exp_f32 v0, v0\n"
    "v_exp_f32 v1, v1\n"
    "v_exp_f32 v2, v2\n"
    "v_exp_f32 v3, v3\n"
    "v_exp_f32 v4, v4\n"
    "v_exp_f32 v5, v5\n"
    "v_exp_f32 v6, v6\n"
    "v_exp_f32 v7, v7\n"
    "v_exp_f32 v8, v8\n"
    "v_exp_f32 v9, v9\n"
    "v_exp_f32 v10, v10\n"
    "v_exp_f32 v11, v11\n"
    "v_exp_f32 v12, v12\n"
    "v_exp_f32 v13, v13\n"
    "v_exp_f32 v14, v14\n"
    "v_exp_f32 v15, v15\n"
    "v_smfmac_f32_32x32x32_bf16 v[32:47], v[64:67], v[0:7], v76\n"
    "s_nop 0\n"
    "v_smfmac_f32_32x32x32_bf16 v[32:47], v[68:71], v[8:15], v76\n"
    "s_cmp_lt_u32 s23, 262144\n"
    "s_cbranch_scc1 f272_%=\n"
    "s_nop 11\n"
    "good70_%=:\n"
    "v_rcp_f32 v78, v40\n"
    "s_waitcnt lgkmcnt(0)\n"
    "v_mfma_f32_32x32x16_bf16 v[0:15], v[96:99], v[128:131], 0\n"
    "v_pk_mul_f32 v[32:33], v[32:33], v[78:79] op_sel:[0,0] op_sel_hi:[1,0]\n"
    "v_pk_mul_f32 v[34:35], v[34:35], v[78:79] op_sel:[0,0] op_sel_hi:[1,0]\n"
    "v_pk_mul_f32 v[36:37], v[36:37], v[78:79] op_sel:[0,0] op_sel_hi:[1,0]\n"
    "v_pk_mul_f32 v[38:39], v[38:39], v[78:79] op_sel:[0,0] op_sel_hi:[1,0]\n"
    "v_cvt_pk_bf16_f32 v48, v32, v33\n"
    "v_mfma_f32_32x32x16_bf16 v[16:31], v[100:103], v[128:131], 0\n"
    "v_cvt_pk_bf16_f32 v49, v34, v35\n"
    "v_cvt_pk_bf16_f32 v50, v36, v37\n"
    "v_cvt_pk_bf16_f32 v51, v38, v39\n"
    "v_min_u32 v48, -2, v48\n"
    "v_min_u32 v49, -2, v49\n"
    "v_mfma_f32_32x32x16_bf16 v[0:15], v[104:107], v[132:135], v[0:15]\n"
    "v_min_u32 v50, -2, v50\n"
    "v_min_u32 v51, -2, v51\n"
    "buffer_store_dwordx4 v[48:51], v255, s[76:79], s20 offen sc1\n"
    "ds_read_b64_tr_b16 v[64:65], v238 offset:32768\n"
    "ds_read_b64_tr_b16 v[66:67], v238 offset:33792\n"
    "ds_read_b64_tr_b16 v[68:69], v239 offset:32768\n"
    "ds_read_b64_tr_b16 v[70:71], v239 offset:33792\n"
    "ds_read_b64_tr_b16 v[72:73], v238 offset:40960\n"
    "ds_read_b64_tr_b16 v[74:75], v238 offset:41984\n"
    "ds_read_b64_tr_b16 v[76:77], v239 offset:40960\n"
    "ds_read_b64_tr_b16 v[78:79], v239 offset:41984\n"
    "v_mfma_f32_32x32x16_bf16 v[16:31], v[108:111], v[132:135], v[16:31]\n"
    "s_cmp_lt_u32 s12, 2\n"
    "s_cselect_b32 s23, -1, 0\n"
    "v_and_b32 v212, s23, v212\n"
    "v_and_b32 v213, s23, v213\n"
    "v_and_b32 v214, s23, v214\n"
    "v_and_b32 v215, s23, v215\n"
    "s_waitcnt lgkmcnt(0)\n"
    "s_barrier\n"
    "v_mfma_f32_32x32x16_bf16 v[0:15], v[64:67], v[136:139], v[0:15]\n"
    "ds_read_b64_tr_b16 v[32:33], v248 offset:0\n"
    "ds_read_b64_tr_b16 v[34:35], v248 offset:1024\n"
    "ds_read_b64_tr_b16 v[36:37], v249 offset:0\n"
    "ds_read_b64_tr_b16 v[38:39], v249 offset:1024\n"
    "v_mfma_f32_32x32x16_bf16 v[16:31], v[68:71], v[136:139], v[16:31]\n"
    "ds_read_b64_tr_b16 v[40:41], v248 offset:8192\n"
    "ds_read_b64_tr_b16 v[42:43], v248 offset:9216\n"
    "ds_read_b64_tr_b16 v[44:45], v249 offset:8192\n"
    "ds_read_b64_tr_b16 v[46:47], v249 offset:9216\n"
    "s_add_u32 m0, s22, 131072\n"
    "s_nop 0\n"
    "buffer_load_dwordx4 v253, s[68:71], s17 offen lds\n"
    "v_mfma_f32_32x32x16_bf16 v[0:15], v[72:75], v[140:143], v[0:15]\n"
    "v_lshlrev_b32 v224, 16, v216\n"
    "v_and_b32 v225, 0xffff0000, v216\n"
    "v_lshlrev_b32 v56, 16, v220\n"
    "v_and_b32 v57, 0xffff0000, v220\n"
    "v_pk_add_f32 v[224:225], v[224:225], v[56:57] op_sel:[0,0] op_sel_hi:[1,1]\n"
    "v_mfma_f32_32x32x16_bf16 v[16:31], v[76:79], v[140:143], v[16:31]\n"
    "s_add_u32 m0, s22, 132096\n"
    "s_nop 0\n"
    "buffer_load_dwordx4 v241, s[68:71], s17 offen lds\n"
    "s_waitcnt lgkmcnt(0)\n"
    "s_barrier\n"
    "v_mfma_f32_32x32x16_bf16 v[0:15], v[32:35], v[144:147], v[0:15]\n"
    "ds_read_b64_tr_b16 v[96:97], v236 offset:32768\n"
    "ds_read_b64_tr_b16 v[98:99], v236 offset:33792\n"
    "ds_read_b64_tr_b16 v[100:101], v237 offset:32768\n"
    "ds_read_b64_tr_b16 v[102:103], v237 offset:33792\n"
    "v_mfma_f32_32x32x16_bf16 v[16:31], v[36:39], v[144:147], v[16:31]\n"
    "ds_read_b64_tr_b16 v[104:105], v236 offset:40960\n"
    "ds_read_b64_tr_b16 v[106:107], v236 offset:41984\n"
    "ds_read_b64_tr_b16 v[108:109], v237 offset:40960\n"
    "ds_read_b64_tr_b16 v[110:111], v237 offset:41984\n"
    "s_add_u32 s17, s17, 131072\n"
    "s_add_u32 m0, s22, 147456\n"
    "s_nop 0\n"
    "buffer_load_dwordx4 v253, s[68:71], s17 offen lds\n"
    "v_mfma_f32_32x32x16_bf16 v[0:15], v[40:43], v[148:151], v[0:15]\n"
    "s_add_u32 m0, s22, 148480\n"
    "s_nop 0\n"
    "buffer_load_dwordx4 v241, s[68:71], s17 offen lds\n"
    "s_add_u32 s17, s17, 131072\n"
    "v_lshlrev_b32 v226, 16, v217\n"
    "v_and_b32 v227, 0xffff0000, v217\n"
    "v_lshlrev_b32 v56, 16, v221\n"
    "v_and_b32 v57, 0xffff0000, v221\n"
    "v_pk_add_f32 v[226:227], v[226:227], v[56:57] op_sel:[0,0] op_sel_hi:[1,1]\n"
    "v_mfma_f32_32x32x16_bf16 v[16:31], v[44:47], v[148:151], v[16:31]\n"
    "s_sub_u32 s21, s17, 0x8000\n"
    "s_cmp_lt_u32 s4, 6\n"
    "s_cselect_b32 s17, s17, s21\n"
    "s_add_u32 m0, s22, 49152\n"
    "s_nop 0\n"
    "buffer_load_dwordx4 v253, s[68:71], s17 offen lds\n"
    "s_waitcnt lgkmcnt(0)\n"
    "v_mfma_f32_32x32x16_bf16 v[0:15], v[96:99], v[152:155], v[0:15]\n"
    "ds_read_b64_tr_b16 v[64:65], v238 offset:16384\n"
    "ds_read_b64_tr_b16 v[66:67], v238 offset:17408\n"
    "ds_read_b64_tr_b16 v[68:69], v239 offset:16384\n"
    "ds_read_b64_tr_b16 v[70:71], v239 offset:17408\n"
    "v_mfma_f32_32x32x16_bf16 v[16:31], v[100:103], v[152:155], v[16:31]\n"
    "ds_read_b64_tr_b16 v[72:73], v238 offset:24576\n"
    "ds_read_b64_tr_b16 v[74:75], v238 offset:25600\n"
    "ds_read_b64_tr_b16 v[76:77], v239 offset:24576\n"
    "ds_read_b64_tr_b16 v[78:79], v239 offset:25600\n"
    "s_add_u32 m0, s22, 50176\n"
    "s_nop 0\n"
    "buffer_load_dwordx4 v241, s[68:71], s17 offen lds\n"
    "v_mfma_f32_32x32x16_bf16 v[0:15], v[104:107], v[156:159], v[0:15]\n"
    "s_add_u32 s17, s17, 131072\n"
    "s_add_u32 m0, s22, 81920\n"
    "s_nop 0\n"
    "buffer_load_dwordx4 v253, s[56:59], s18 offen lds\n"
    "v_lshlrev_b32 v228, 16, v218\n"
    "v_and_b32 v229, 0xffff0000, v218\n"
    "v_lshlrev_b32 v56, 16, v222\n"
    "v_and_b32 v57, 0xffff0000, v222\n"
    "v_pk_add_f32 v[228:229], v[228:229], v[56:57] op_sel:[0,0] op_sel_hi:[1,1]\n"
    "v_mfma_f32_32x32x16_bf16 v[16:31], v[108:111], v[156:159], v[16:31]\n"
    "s_add_u32 m0, s22, 82944\n"
    "s_nop 0\n"
    "buffer_load_dwordx4 v241, s[56:59], s18 offen lds\n"
    "s_waitcnt lgkmcnt(0)\n"
    "v_mfma_f32_32x32x16_bf16 v[0:15], v[64:67], v[160:163], v[0:15]\n"
    "ds_read_b64_tr_b16 v[32:33], v238 offset:49152\n"
    "ds_read_b64_tr_b16 v[34:35], v238 offset:50176\n"
    "ds_read_b64_tr_b16 v[36:37], v239 offset:49152\n"
    "ds_read_b64_tr_b16 v[38:39], v239 offset:50176\n"
    "v_mfma_f32_32x32x16_bf16 v[16:31], v[68:71], v[160:163], v[16:31]\n"
    "ds_read_b64_tr_b16 v[40:41], v238 offset:57344\n"
    "ds_read_b64_tr_b16 v[42:43], v238 offset:58368\n"
    "ds_read_b64_tr_b16 v[44:45], v239 offset:57344\n"
    "ds_read_b64_tr_b16 v[46:47], v239 offset:58368\n"
    "s_add_u32 s18, s18, 32768\n"
    "s_add_u32 m0, s22, 114688\n"
    "s_nop 0\n"
    "buffer_load_dwordx4 v253, s[56:59], s18 offen lds\n"
    "v_mfma_f32_32x32x16_bf16 v[0:15], v[72:75], v[164:167], v[0:15]\n"
    "v_lshlrev_b32 v230, 16, v219\n"
    "v_and_b32 v231, 0xffff0000, v219\n"
    "v_lshlrev_b32 v56, 16, v223\n"
    "v_and_b32 v57, 0xffff0000, v223\n"
    "v_pk_add_f32 v[230:231], v[230:231], v[56:57] op_sel:[0,0] op_sel_hi:[1,1]\n"
    "v_mfma_f32_32x32x16_bf16 v[16:31], v[76:79], v[164:167], v[16:31]\n"
    "s_add_u32 m0, s22, 115712\n"
    "s_nop 0\n"
    "buffer_load_dwordx4 v241, s[56:59], s18 offen lds\n"
    "s_add_u32 s18, s18, 32768\n"
    "s_waitcnt vmcnt(6) lgkmcnt(0)\n"
    "s_barrier\n"
    "v_mfma_f32_32x32x16_bf16 v[0:15], v[32:35], v[168:171], v[0:15]\n"
    "ds_read_b64_tr_b16 v[96:97], v236 offset:0\n"
    "ds_read_b64_tr_b16 v[98:99], v236 offset:1024\n"
    "ds_read_b64_tr_b16 v[100:101], v237 offset:0\n"
    "ds_read_b64_tr_b16 v[102:103], v237 offset:1024\n"
    "v_mfma_f32_32x32x16_bf16 v[16:31], v[36:39], v[168:171], v[16:31]\n"
    "ds_read_b64_tr_b16 v[104:105], v236 offset:8192\n"
    "ds_read_b64_tr_b16 v[106:107], v236 offset:9216\n"
    "ds_read_b64_tr_b16 v[108:109], v237 offset:8192\n"
    "ds_read_b64_tr_b16 v[110:111], v237 offset:9216\n"
    "buffer_load_dwordx4 v[80:83], v255, s[76:79], s19 offen offset:0 sc1\n"
    "v_mfma_f32_32x32x16_bf16 v[0:15], v[40:43], v[172:175], v[0:15]\n"
    "s_xor_b32 s21, s4, 2\n"
    "s_lshl_b32 s21, s21, 11\n"
    "s_add_u32 s21, s21, 0\n"
    "v_add_u32 v49, s21, v255\n"
    "v_mfma_f32_32x32x16_bf16 v[16:31], v[44:47], v[172:175], v[16:31]\n"
    "buffer_load_dwordx4 v[84:87], v255, s[76:79], s19 offen offset:1024 sc1\n"
    "s_waitcnt lgkmcnt(0)\n"
    "v_mfma_f32_32x32x16_bf16 v[0:15], v[96:99], v[176:179], v[0:15]\n"
    "ds_read_b64_tr_b16 v[64:65], v236 offset:16384\n"
    "ds_read_b64_tr_b16 v[66:67], v236 offset:17408\n"
    "ds_read_b64_tr_b16 v[68:69], v237 offset:16384\n"
    "ds_read_b64_tr_b16 v[70:71], v237 offset:17408\n"
    "v_mfma_f32_32x32x16_bf16 v[16:31], v[100:103], v[176:179], v[16:31]\n"
    "ds_read_b64_tr_b16 v[72:73], v236 offset:24576\n"
    "ds_read_b64_tr_b16 v[74:75], v236 offset:25600\n"
    "ds_read_b64_tr_b16 v[76:77], v237 offset:24576\n"
    "ds_read_b64_tr_b16 v[78:79], v237 offset:25600\n"
    "buffer_load_dwordx4 v[88:91], v255, s[76:79], s19 offen offset:2048 sc1\n"
    "v_mfma_f32_32x32x16_bf16 v[0:15], v[104:107], v[180:183], v[0:15]\n"
    "s_xor_b32 s21, s4, 4\n"
    "s_lshl_b32 s21, s21, 11\n"
    "s_add_u32 s21, s21, 16384\n"
    "v_add_u32 v50, s21, v255\n"
    "v_mfma_f32_32x32x16_bf16 v[16:31], v[108:111], v[180:183], v[16:31]\n"
    "buffer_load_dwordx4 v[92:95], v255, s[76:79], s19 offen offset:3072 sc1\n"
    "s_waitcnt lgkmcnt(0)\n"
    "v_mfma_f32_32x32x16_bf16 v[0:15], v[64:67], v[184:187], v[0:15]\n"
    "ds_read_b64_tr_b16 v[32:33], v248 offset:16384\n"
    "ds_read_b64_tr_b16 v[34:35], v248 offset:17408\n"
    "ds_read_b64_tr_b16 v[36:37], v249 offset:16384\n"
    "ds_read_b64_tr_b16 v[38:39], v249 offset:17408\n"
    "v_mfma_f32_32x32x16_bf16 v[16:31], v[68:71], v[184:187], v[16:31]\n"
    "ds_read_b64_tr_b16 v[40:41], v248 offset:24576\n"
    "ds_read_b64_tr_b16 v[42:43], v248 offset:25600\n"
    "ds_read_b64_tr_b16 v[44:45], v249 offset:24576\n"
    "ds_read_b64_tr_b16 v[46:47], v249 offset:25600\n"
    "buffer_load_dwordx4 v[112:115], v255, s[76:79], s37 offen offset:0 sc1\n"
    "v_mfma_f32_32x32x16_bf16 v[0:15], v[72:75], v[188:191], v[0:15]\n"
    "s_xor_b32 s21, s4, 6\n"
    "s_lshl_b32 s21, s21, 11\n"
    "s_add_u32 s21, s21, 131072\n"
    "v_add_u32 v51, s21, v255\n"
    "v_mfma_f32_32x32x16_bf16 v[16:31], v[76:79], v[188:191], v[16:31]\n"
    "buffer_load_dwordx4 v[116:119], v255, s[76:79], s37 offen offset:1024 sc1\n"
    "s_waitcnt lgkmcnt(0)\n"
    "v_mfma_f32_32x32x16_bf16 v[0:15], v[32:35], v[192:195], v[0:15]\n"
    "ds_read_b64_tr_b16 v[96:97], v248 offset:32768\n"
    "ds_read_b64_tr_b16 v[98:99], v248 offset:33792\n"
    "ds_read_b64_tr_b16 v[100:101], v249 offset:32768\n"
    "ds_read_b64_tr_b16 v[102:103], v249 offset:33792\n"
    "v_mfma_f32_32x32x16_bf16 v[16:31], v[36:39], v[192:195], v[16:31]\n"
    "ds_read_b64_tr_b16 v[104:105], v248 offset:40960\n"
    "ds_read_b64_tr_b16 v[106:107], v248 offset:41984\n"
    "ds_read_b64_tr_b16 v[108:109], v249 offset:40960\n"
    "ds_read_b64_tr_b16 v[110:111], v249 offset:41984\n"
    "buffer_load_dwordx4 v[120:123], v255, s[76:79], s37 offen offset:2048 sc1\n"
    "v_mfma_f32_32x32x16_bf16 v[0:15], v[40:43], v[196:199], v[0:15]\n"
    "v_add_u32 v48, s22, v255\n"
    "v_add_u32 v53, 131072, v48\n"
    "v_and_b32 v52, 8, v240\n"
    "v_add_u32 v240, v240, v52\n"
    "v_mfma_f32_32x32x16_bf16 v[16:31], v[44:47], v[196:199], v[16:31]\n"
    "buffer_load_dwordx4 v[124:127], v255, s[76:79], s37 offen offset:3072 sc1\n"
    "s_waitcnt vmcnt(8) lgkmcnt(0)\n"
    "s_barrier\n"
    "v_mfma_f32_32x32x16_bf16 v[0:15], v[96:99], v[200:203], v[0:15]\n"
    "ds_read_b64_tr_b16 v[64:65], v238 offset:0\n"
    "ds_read_b64_tr_b16 v[66:67], v238 offset:1024\n"
    "ds_read_b64_tr_b16 v[68:69], v239 offset:0\n"
    "ds_read_b64_tr_b16 v[70:71], v239 offset:1024\n"
    "v_mfma_f32_32x32x16_bf16 v[16:31], v[100:103], v[200:203], v[16:31]\n"
    "ds_read_b64_tr_b16 v[72:73], v238 offset:8192\n"
    "ds_read_b64_tr_b16 v[74:75], v238 offset:9216\n"
    "ds_read_b64_tr_b16 v[76:77], v239 offset:8192\n"
    "ds_read_b64_tr_b16 v[78:79], v239 offset:9216\n"
    "s_add_u32 m0, s22, 32768\n"
    "s_nop 0\n"
    "buffer_load_dwordx4 v253, s[56:59], s18 offen lds\n"
    "v_mfma_f32_32x32x16_bf16 v[0:15], v[104:107], v[204:207], v[0:15]\n"
    "v_mfma_f32_32x32x16_bf16 v[16:31], v[108:111], v[204:207], v[16:31]\n"
    "s_add_u32 m0, s22, 33792\n"
    "s_nop 0\n"
    "buffer_load_dwordx4 v241, s[56:59], s18 offen lds\n"
    "s_waitcnt lgkmcnt(0)\n"
    "v_mfma_f32_32x32x16_bf16 v[0:15], v[64:67], v[208:211], v[0:15]\n"
    "ds_read_b64_tr_b16 v[32:33], v238 offset:32768\n"
    "ds_read_b64_tr_b16 v[34:35], v238 offset:33792\n"
    "ds_read_b64_tr_b16 v[36:37], v239 offset:32768\n"
    "ds_read_b64_tr_b16 v[38:39], v239 offset:33792\n"
    "v_mfma_f32_32x32x16_bf16 v[16:31], v[68:71], v[208:211], v[16:31]\n"
    "ds_read_b64_tr_b16 v[40:41], v238 offset:40960\n"
    "ds_read_b64_tr_b16 v[42:43], v238 offset:41984\n"
    "ds_read_b64_tr_b16 v[44:45], v239 offset:40960\n"
    "ds_read_b64_tr_b16 v[46:47], v239 offset:41984\n"
    "s_add_u32 s18, s18, 32768\n"
    "s_add_u32 m0, s22, 65536\n"
    "s_nop 0\n"
    "buffer_load_dwordx4 v253, s[56:59], s18 offen lds\n"
    "s_waitcnt vmcnt(3)\n"
    "v_mfma_f32_32x32x16_bf16 v[0:15], v[72:75], v[212:215], v[0:15]\n"
    "v_max3_u32 v54, v80, v81, v82\n"
    "v_max3_u32 v55, v83, v84, v85\n"
    "v_max3_u32 v54, v54, v86, v87\n"
    "v_max3_u32 v55, v55, v88, v89\n"
    "v_max3_u32 v54, v54, v90, v91\n"
    "v_max3_u32 v55, v55, v92, v93\n"
    "v_max3_u32 v54, v54, v94, v95\n"
    "v_max3_u32 v55, v55, v112, v113\n"
    "v_mfma_f32_32x32x16_bf16 v[16:31], v[76:79], v[212:215], v[16:31]\n"
    "v_max3_u32 v54, v54, v114, v115\n"
    "v_max3_u32 v55, v55, v116, v117\n"
    "v_max3_u32 v54, v54, v118, v119\n"
    "v_max3_u32 v55, v55, v120, v121\n"
    "v_max3_u32 v54, v54, v122, v123\n"
    "v_max3_u32 v55, v55, v124, v125\n"
    "v_max3_u32 v54, v54, v126, v127\n"
    "v_max_u32 v54, v54, v55\n"
    "v_cmp_eq_u32 s[26:27], -1, v54\n"
    "s_add_u32 m0, s22, 66560\n"
    "s_nop 0\n"
    "buffer_load_dwordx4 v241, s[56:59], s18 offen lds\n"
    "s_add_u32 s18, s18, 32768\n"
    "s_waitcnt lgkmcnt(0)\n"
    "s_mov_b32 s24, 1\n"
    "s_cmp_eq_u64 s[26:27], 0\n"
    "s_cbranch_scc1 oo74_%=\n"
    "os73_%=:\n"
    "buffer_load_dwordx4 v[80:83], v255, s[76:79], s19 offen offset:0 sc1\n"
    "buffer_load_dwordx4 v[84:87], v255, s[76:79], s19 offen offset:1024 sc1\n"
    "buffer_load_dwordx4 v[88:91], v255, s[76:79], s19 offen offset:2048 sc1\n"
    "buffer_load_dwordx4 v[92:95], v255, s[76:79], s19 offen offset:3072 sc1\n"
    "buffer_load_dwordx4 v[112:115], v255, s[76:79], s37 offen offset:0 sc1\n"
    "buffer_load_dwordx4 v[116:119], v255, s[76:79], s37 offen offset:1024 sc1\n"
    "buffer_load_dwordx4 v[120:123], v255, s[76:79], s37 offen offset:2048 sc1\n"
    "buffer_load_dwordx4 v[124:127], v255, s[76:79], s37 offen offset:3072 sc1\n"
    "s_add_u32 s24, s24, 1\n"
    "s_waitcnt vmcnt(0)\n"
    "v_max3_u32 v54, v80, v81, v82\n"
    "v_max3_u32 v55, v83, v84, v85\n"
    "v_max3_u32 v54, v54, v86, v87\n"
    "v_max3_u32 v55, v55, v88, v89\n"
    "v_max3_u32 v54, v54, v90, v91\n"
    "v_max3_u32 v55, v55, v92, v93\n"
    "v_max3_u32 v54, v54, v94, v95\n"
    "v_max3_u32 v55, v55, v112, v113\n"
    "v_max3_u32 v54, v54, v114, v115\n"
    "v_max3_u32 v55, v55, v116, v117\n"
    "v_max3_u32 v54, v54, v118, v119\n"
    "v_max3_u32 v55, v55, v120, v121\n"
    "v_max3_u32 v54, v54, v122, v123\n"
    "v_max3_u32 v55, v55, v124, v125\n"
    "v_max3_u32 v54, v54, v126, v127\n"
    "v_max_u32 v54, v54, v55\n"
    "v_cmp_eq_u32 s[26:27], -1, v54\n"
    "s_cmp_gt_u32 s24, 0x20000\n"
    "s_cbranch_scc1 oo74_%=\n"
    "s_cmp_eq_u64 s[26:27], 0\n"
    "s_cbranch_scc0 os73_%=\n"
    "oo74_%=:\n"
    "v_mfma_f32_32x32x16_bf16 v[0:15], v[32:35], v[80:83], v[0:15]\n"
    "ds_read_b64_tr_b16 v[96:97], v248 offset:0\n"
    "ds_read_b64_tr_b16 v[98:99], v248 offset:1024\n"
    "ds_read_b64_tr_b16 v[100:101], v249 offset:0\n"
    "ds_read_b64_tr_b16 v[102:103], v249 offset:1024\n"
    "v_mfma_f32_32x32x16_bf16 v[16:31], v[36:39], v[80:83], v[16:31]\n"
    "ds_read_b64_tr_b16 v[104:105], v248 offset:8192\n"
    "ds_read_b64_tr_b16 v[106:107], v248 offset:9216\n"
    "ds_read_b64_tr_b16 v[108:109], v249 offset:8192\n"
    "ds_read_b64_tr_b16 v[110:111], v249 offset:9216\n"
    "v_mfma_f32_32x32x16_bf16 v[0:15], v[40:43], v[84:87], v[0:15]\n"
    "v_mfma_f32_32x32x16_bf16 v[16:31], v[44:47], v[84:87], v[16:31]\n"
    "s_waitcnt vmcnt(2) lgkmcnt(0)\n"
    "s_barrier\n"
    "v_mfma_f32_32x32x16_bf16 v[0:15], v[96:99], v[88:91], v[0:15]\n"
    "ds_read_b64_tr_b16 v[64:65], v236 offset:32768\n"
    "ds_read_b64_tr_b16 v[66:67], v236 offset:33792\n"
    "ds_read_b64_tr_b16 v[68:69], v237 offset:32768\n"
    "ds_read_b64_tr_b16 v[70:71], v237 offset:33792\n"
    "v_mfma_f32_32x32x16_bf16 v[16:31], v[100:103], v[88:91], v[16:31]\n"
    "ds_read_b64_tr_b16 v[72:73], v236 offset:40960\n"
    "ds_read_b64_tr_b16 v[74:75], v236 offset:41984\n"
    "ds_read_b64_tr_b16 v[76:77], v237 offset:40960\n"
    "ds_read_b64_tr_b16 v[78:79], v237 offset:41984\n"
    "v_mfma_f32_32x32x16_bf16 v[0:15], v[104:107], v[92:95], v[0:15]\n"
    "v_mfma_f32_32x32x16_bf16 v[16:31], v[108:111], v[92:95], v[16:31]\n"
    "s_waitcnt vmcnt(0) lgkmcnt(0)\n"
    "s_barrier\n"
    "v_mfma_f32_32x32x16_bf16 v[0:15], v[64:67], v[112:115], v[0:15]\n"
    "ds_read_b64_tr_b16 v[32:33], v238 offset:16384\n"
    "ds_read_b64_tr_b16 v[34:35], v238 offset:17408\n"
    "ds_read_b64_tr_b16 v[36:37], v239 offset:16384\n"
    "ds_read_b64_tr_b16 v[38:39], v239 offset:17408\n"
    "v_mfma_f32_32x32x16_bf16 v[16:31], v[68:71], v[112:115], v[16:31]\n"
    "ds_read_b64_tr_b16 v[40:41], v238 offset:24576\n"
    "ds_read_b64_tr_b16 v[42:43], v238 offset:25600\n"
    "ds_read_b64_tr_b16 v[44:45], v239 offset:24576\n"
    "ds_read_b64_tr_b16 v[46:47], v239 offset:25600\n"
    "v_mfma_f32_32x32x16_bf16 v[0:15], v[72:75], v[116:119], v[0:15]\n"
    "v_mfma_f32_32x32x16_bf16 v[16:31], v[76:79], v[116:119], v[16:31]\n"
    "s_waitcnt lgkmcnt(0)\n"
    "v_mfma_f32_32x32x16_bf16 v[0:15], v[32:35], v[120:123], v[0:15]\n"
    "v_mfma_f32_32x32x16_bf16 v[0:15], v[40:43], v[124:127], v[0:15]\n"
    "v_mfma_f32_32x32x16_bf16 v[16:31], v[36:39], v[120:123], v[16:31]\n"
    "v_mfma_f32_32x32x16_bf16 v[16:31], v[44:47], v[124:127], v[16:31]\n"
    "s_nop 9\n"
    "ds_write_b128 v49, v[8:11]\n"
    "ds_write_b128 v49, v[12:15] offset:1024\n"
    "ds_write_b128 v50, v[16:19]\n"
    "ds_write_b128 v50, v[20:23] offset:1024\n"
    "ds_write_b128 v51, v[24:27]\n"
    "ds_write_b128 v51, v[28:31] offset:1024\n"
    "s_waitcnt lgkmcnt(0)\n"
    "s_barrier\n"
    "ds_read_b128 v[128:131], v48 offset:0\n"
    "ds_read_b128 v[132:135], v48 offset:1024\n"
    "ds_read_b128 v[136:139], v48 offset:16384\n"
    "ds_read_b128 v[140:143], v48 offset:17408\n"
    "ds_read_b128 v[144:147], v53 offset:0\n"
    "ds_read_b128 v[148:151], v53 offset:1024\n"
    "v_pk_add_f32 v[0:1], v[0:1], v[224:225] op_sel:[0,0] op_sel_hi:[1,1]\n"
    "v_pk_add_f32 v[2:3], v[2:3], v[226:227] op_sel:[0,0] op_sel_hi:[1,1]\n"
    "v_pk_add_f32 v[4:5], v[4:5], v[228:229] op_sel:[0,0] op_sel_hi:[1,1]\n"
    "v_pk_add_f32 v[6:7], v[6:7], v[230:231] op_sel:[0,0] op_sel_hi:[1,1]\n"
    "s_waitcnt lgkmcnt(4)\n"
    "v_pk_add_f32 v[0:1], v[0:1], v[128:129] op_sel:[0,0] op_sel_hi:[1,1]\n"
    "v_pk_add_f32 v[2:3], v[2:3], v[130:131] op_sel:[0,0] op_sel_hi:[1,1]\n"
    "v_pk_add_f32 v[4:5], v[4:5], v[132:133] op_sel:[0,0] op_sel_hi:[1,1]\n"
    "v_pk_add_f32 v[6:7], v[6:7], v[134:135] op_sel:[0,0] op_sel_hi:[1,1]\n"
    "s_waitcnt lgkmcnt(2)\n"
    "v_pk_add_f32 v[0:1], v[0:1], v[136:137] op_sel:[0,0] op_sel_hi:[1,1]\n"
    "v_pk_add_f32 v[2:3], v[2:3], v[138:139] op_sel:[0,0] op_sel_hi:[1,1]\n"
    "v_pk_add_f32 v[4:5], v[4:5], v[140:141] op_sel:[0,0] op_sel_hi:[1,1]\n"
    "v_pk_add_f32 v[6:7], v[6:7], v[142:143] op_sel:[0,0] op_sel_hi:[1,1]\n"
    "s_waitcnt lgkmcnt(0)\n"
    "v_pk_add_f32 v[0:1], v[0:1], v[144:145] op_sel:[0,0] op_sel_hi:[1,1]\n"
    "v_pk_add_f32 v[2:3], v[2:3], v[146:147] op_sel:[0,0] op_sel_hi:[1,1]\n"
    "v_pk_add_f32 v[4:5], v[4:5], v[148:149] op_sel:[0,0] op_sel_hi:[1,1]\n"
    "v_pk_add_f32 v[6:7], v[6:7], v[150:151] op_sel:[0,0] op_sel_hi:[1,1]\n"
    "v_cvt_pk_bf16_f32 v8, v0, v1\n"
    "v_cvt_pk_bf16_f32 v9, v2, v3\n"
    "v_cvt_pk_bf16_f32 v10, v4, v5\n"
    "v_cvt_pk_bf16_f32 v11, v6, v7\n"
    "s_nop 0\n"
    "v_permlane32_swap_b32 v8, v10\n"
    "v_permlane32_swap_b32 v9, v11\n"
    "buffer_store_dwordx4 v[8:11], v240, s[80:83], s38 offen\n"
    : "+{v0}"(tid)
    : "{s[0:1]}"(__builtin_amdgcn_kernarg_segment_ptr()), "{s2}"(blockIdx.x)
    : "memory", "scc", "vcc", "v1", "v2", "v3", "v4", "v5", "v6", "v7", "v8", "v9", "v10", "v11", "v12", "v13", "v14", "v15", "v16", "v17", "v18", "v19", "v20", "v21", "v22", "v23", "v24", "v25", "v26", "v27", "v28", "v29", "v30", "v31", "v32", "v33", "v34", "v35", "v36", "v37", "v38", "v39", "v40", "v41", "v42", "v43", "v44", "v45", "v46", "v47", "v48", "v49", "v50", "v51", "v52", "v53", "v54", "v55", "v56", "v57", "v58", "v59", "v60", "v61", "v62", "v63", "v64", "v65", "v66", "v67", "v68", "v69", "v70", "v71", "v72", "v73", "v74", "v75", "v76", "v77", "v78", "v79", "v80", "v81", "v82", "v83", "v84", "v85", "v86", "v87", "v88", "v89", "v90", "v91", "v92", "v93", "v94", "v95", "v96", "v97", "v98", "v99", "v100", "v101", "v102", "v103", "v104", "v105", "v106", "v107", "v108", "v109", "v110", "v111", "v112", "v113", "v114", "v115", "v116", "v117", "v118", "v119", "v120", "v121", "v122", "v123", "v124", "v125", "v126", "v127", "v128", "v129", "v130", "v131", "v132", "v133", "v134", "v135", "v136", "v137", "v138", "v139", "v140", "v141", "v142", "v143", "v144", "v145", "v146", "v147", "v148", "v149", "v150", "v151", "v152", "v153", "v154", "v155", "v156", "v157", "v158", "v159", "v160", "v161", "v162", "v163", "v164", "v165", "v166", "v167", "v168", "v169", "v170", "v171", "v172", "v173", "v174", "v175", "v176", "v177", "v178", "v179", "v180", "v181", "v182", "v183", "v184", "v185", "v186", "v187", "v188", "v189", "v190", "v191", "v192", "v193", "v194", "v195", "v196", "v197", "v198", "v199", "v200", "v201", "v202", "v203", "v204", "v205", "v206", "v207", "v208", "v209", "v210", "v211", "v212", "v213", "v214", "v215", "v216", "v217", "v218", "v219", "v220", "v221", "v222", "v223", "v224", "v225", "v226", "v227", "v228", "v229", "v230", "v231", "v232", "v233", "v234", "v235", "v236", "v237", "v238", "v239", "v240", "v241", "v242", "v243", "v244", "v245", "v246", "v247", "v248", "v249", "v250", "v251", "v252", "v253", "v254", "v255", "s3", "s4", "s5", "s6", "s7", "s8", "s9", "s10", "s11", "s12", "s13", "s14", "s15", "s16", "s17", "s18", "s19", "s20", "s21", "s22", "s23", "s24", "s25", "s26", "s27", "s28", "s29", "s30", "s31", "s34", "s35", "s36", "s37", "s38", "s39", "s40", "s41", "s42", "s43", "s44", "s45", "s46", "s47", "s48", "s49", "s50", "s51", "s52", "s53", "s54", "s55", "s56", "s57", "s58", "s59", "s60", "s61", "s62", "s63", "s64", "s65", "s66", "s67", "s68", "s69", "s70", "s71", "s72", "s73", "s74", "s75", "s76", "s77", "s78", "s79", "s80", "s81", "s82", "s83", "s84", "s85", "s86", "s87", "s88", "s89", "s90", "s91", "s92", "s93", "s94", "s95", "s96", "s97", "s98", "s99");
}

extern "C" void kernel_launch(void* const* d_in, const int* in_sizes, int n_in, void* d_out, int out_size, void* d_ws, size_t ws_size,
                              hipStream_t stream) {
  constexpr int LDS_BYTES = 163840;
  (void)hipFuncSetAttribute((const void*)fused_block, hipFuncAttributeMaxDynamicSharedMemorySize, LDS_BYTES);
  fused_block<<<256, 512, LDS_BYTES, stream>>>(d_in[0], d_in[1], d_in[2], d_in[3], d_in[4], d_in[6], d_in[7], d_in[8], d_in[9], d_ws, d_out);
}
```
